```python
import torch

def _softmax(x, axis=-1):
    return torch.softmax(x, dim=axis)
def _gelu(x):
    return torch.nn.functional.gelu(x, approximate="tanh")
def _silu(x):
    return torch.nn.functional.silu(x)
def _sigmoid(x):
    return torch.sigmoid(x)
def _layernorm(x, axis=-1, eps=1e-5):
    m = x.mean(dim=axis, keepdim=True)
    v = x.var(dim=axis, keepdim=True, unbiased=False)
    return (x - m) / torch.sqrt(v + eps)
def _scatter_add(tgt, idx, src, axis=0):
    return tgt.clone().index_add_(axis, idx, src)
def _segment_sum(src, seg, n):
    out = torch.zeros((n,) + src.shape[1:], dtype=src.dtype)
    return out.index_add_(0, seg, src)
def _pad(x, pw):
    flat = tuple(v for pair in reversed(pw) for v in pair)
    return torch.nn.functional.pad(x, flat)

kernel_name = 'recurrent_scan__ssm_conv_s5__glu_gated_bilinear__prompt'

def setup_inputs():

    _w = (lambda a: (lambda t: t.to(torch.bfloat16) if t.is_floating_point() else t)(torch.as_tensor(a)))
    def _A_setup():
        B, S, D = 1, 2048, 448
        P = D
        Lambda_re = -0.5 * (lambda _x: torch.exp(torch.as_tensor(_x)))((lambda *s: torch.randn(*s, dtype=torch.bfloat16) * 0.8)(P))
        Lambda_im = (lambda *s: torch.randn(*s, dtype=torch.bfloat16) * 0.8)(P) * 1.0
        B_re = (lambda *s: torch.randn(*s, dtype=torch.bfloat16) * 1.6)(P, D) * (D ** -0.5)
        B_im = (lambda *s: torch.randn(*s, dtype=torch.bfloat16) * 1.6)(P, D) * (D ** -0.5)
        C_re = (lambda *s: torch.randn(*s, dtype=torch.bfloat16) * 1.6)(D, P) * (P ** -0.5)
        C_im = (lambda *s: torch.randn(*s, dtype=torch.bfloat16) * 1.6)(D, P) * (P ** -0.5)
        log_dt = (lambda *s: torch.randn(*s, dtype=torch.bfloat16) * 0.8)(P) * 0.3 - 2.0
        Dskip = (lambda *s: torch.randn(*s, dtype=torch.bfloat16) * 0.8)(D)
        return {
            "x": _w((lambda *s: torch.randn(*s, dtype=torch.bfloat16) * 0.05)(B, S, D)),
            "Lambda_re": _w(Lambda_re),
            "Lambda_im": _w(Lambda_im),
            "B_re": _w(B_re),
            "B_im": _w(B_im),
            "C_re": _w(C_re),
            "C_im": _w(C_im),
            "log_dt": _w(log_dt),
            "Dskip": _w(Dskip),
        }
    _A_in = _A_setup()

    def _B_setup():
        B, S, D, F = 1, 2048, 448, 1792
        return {
            "x": _w((lambda *s: torch.randn(*s, dtype=torch.bfloat16) * 0.05)(B, S, D)),
            "w_gate": _w((lambda *s: torch.randn(*s, dtype=torch.bfloat16) * 0.2)(D, F) * (D ** -0.5)),
            "w_up": _w((lambda *s: torch.randn(*s, dtype=torch.bfloat16) * 0.2)(D, F) * (D ** -0.5)),
            "w_down": _w((lambda *s: torch.randn(*s, dtype=torch.bfloat16) * 0.2)(F, D) * (F ** -0.5)),
        }
    _B_in = _B_setup()

    return {
        "A_x": _A_in["x"],
        "A_Lambda_re": _A_in["Lambda_re"],
        "A_Lambda_im": _A_in["Lambda_im"],
        "A_B_re": _A_in["B_re"],
        "A_B_im": _A_in["B_im"],
        "A_C_re": _A_in["C_re"],
        "A_C_im": _A_in["C_im"],
        "A_log_dt": _A_in["log_dt"],
        "A_Dskip": _A_in["Dskip"],
        "B_x": _B_in["x"],
        "B_w_gate": _B_in["w_gate"],
        "B_w_up": _B_in["w_up"],
        "B_w_down": _B_in["w_down"],
    }

def reference(A_x, A_Lambda_re, A_Lambda_im, A_B_re, A_B_im, A_C_re, A_C_im, A_log_dt, A_Dskip, B_x, B_w_gate, B_w_up, B_w_down):
    def _A_ref(x, Lambda_re, Lambda_im, B_re, B_im, C_re, C_im, log_dt, Dskip):
        Bsz, L, H = x.shape
        Lam = Lambda_re + 1j * Lambda_im
        Bmat = B_re + 1j * B_im
        Cmat = C_re + 1j * C_im
        dt = torch.exp(log_dt)
        Lam_bar = torch.exp(dt * Lam)
        B_bar = ((Lam_bar - 1.0) / Lam)[:, None] * Bmat
        Bu = torch.einsum("bld,pd->blp", x + 0j, B_bar)
        h = torch.zeros_like(Bu[:, 0, :])
        hs = []
        for t in range(L):
            h = Lam_bar[None, :] * h + Bu[:, t, :]
            hs.append(h)
        hseq = torch.stack(hs, 1)
        y = 2.0 * torch.einsum("blp,dp->bld", hseq, Cmat).real + x * Dskip[None, None, :]
        return y

    def _B_ref(x, w_gate, w_up, w_down):
        g = torch.einsum("bsd,df->bsf", x, w_gate)
        u = torch.einsum("bsd,df->bsf", x, w_up)
        h = g * u
        return torch.einsum("bsf,fd->bsd", h, w_down)

    x = A_x
    xn = _layernorm(x, axis=-1)
    s = _A_ref(xn, A_Lambda_re, A_Lambda_im, A_B_re, A_B_im, A_C_re, A_C_im, A_log_dt, A_Dskip)
    g = _silu(_B_ref(xn, B_w_gate, B_w_up, B_w_down))
    return x + s * g
```

```cpp
#include <hip/hip_runtime.h>
#include <hip/hip_bf16.h>
typedef unsigned short u16;
constexpr int L=2048,D=448,P=448,F=1792;
__device__ inline float b2f(u16 v){return __uint_as_float((unsigned)v<<16);}
__device__ inline float rbf(float f){unsigned u=__float_as_uint(f);u+=0x7fffu+((u>>16)&1u);return __uint_as_float(u&0xffff0000u);}
__global__ void k_ln(const u16*x,float*xn){
  int t=blockIdx.x,l=threadIdx.x;float v[7];float s=0;
  for(int i=0;i<7;i++){v[i]=b2f(x[t*D+l+64*i]);s+=v[i];}
  for(int o=32;o;o>>=1)s+=__shfl_xor(s,o);
  float mean=s/D;float q=0;for(int i=0;i<7;i++){float d=v[i]-mean;q+=d*d;}
  for(int o=32;o;o>>=1)q+=__shfl_xor(q,o);
  float m=rbf(mean),var=rbf(q/D),sd=rbf(sqrtf(rbf(var+1e-5f)));
  for(int i=0;i<7;i++)xn[t*D+l+64*i]=rbf(rbf(v[i]-m)/sd);
}
__global__ void k_bu(const float*xn,const u16*Bre,const u16*Bim,const u16*lre,const u16*lim,const u16*ldt,float2*bu){
  int t=blockIdx.x,p=threadIdx.x;__shared__ float xs[D];xs[p]=xn[t*D+p];__syncthreads();
  float a=0,b=0;for(int d=0;d<D;d++){a+=xs[d]*b2f(Bre[p*D+d]);b+=xs[d]*b2f(Bim[p*D+d]);}
  float dt=expf(b2f(ldt[p]));
  dt=rbf(dt);
  float lr=b2f(lre[p]),li=b2f(lim[p]);float e=expf(dt*lr);float br=e*cosf(dt*li),bi=e*sinf(dt*li);
  float nr=br-1.f,ni=bi,den=lr*lr+li*li;float cr=(nr*lr+ni*li)/den,ci=(ni*lr-nr*li)/den;
  bu[t*P+p]=make_float2(cr*a-ci*b,cr*b+ci*a);
}
__global__ void k_scan(float2*bu,const u16*lre,const u16*lim,const u16*ldt){
  int p=blockIdx.x*64+threadIdx.x;float dt=expf(b2f(ldt[p]));
  dt=rbf(dt);
  float lr=b2f(lre[p]),li=b2f(lim[p]);float e=expf(dt*lr);float br=e*cosf(dt*li),bi=e*sinf(dt*li);
  float hr=0,hi=0;for(int t=0;t<L;t++){float2 v=bu[t*P+p];float nr=br*hr-bi*hi+v.x,ni=br*hi+bi*hr+v.y;hr=nr;hi=ni;bu[t*P+p]=make_float2(hr,hi);}
}
__global__ void k_h(const float*xn,const u16*wg,const u16*wu,float*h){
  int t=blockIdx.x;__shared__ float xs[D];xs[threadIdx.x]=xn[t*D+threadIdx.x];__syncthreads();
  for(int f=threadIdx.x;f<F;f+=D){float a=0,b=0;for(int d=0;d<D;d++){a+=xs[d]*b2f(wg[d*F+f]);b+=xs[d]*b2f(wu[d*F+f]);}
    h[t*F+f]=rbf(rbf(a)*rbf(b));
  }
}
__global__ void k_out(const u16*x,const float*xn,const float2*hs,const float*h,const u16*Cre,const u16*Cim,const u16*wd,const u16*dsk,float*out){
  int t=blockIdx.x,d=threadIdx.x;__shared__ float hh[F];__shared__ float2 ss[P];
  for(int f=d;f<F;f+=D)hh[f]=h[t*F+f];ss[d]=hs[t*P+d];__syncthreads();
  float y=0;for(int p=0;p<P;p++)y+=ss[p].x*b2f(Cre[d*P+p])-ss[p].y*b2f(Cim[d*P+p]);
  float o=0;for(int f=0;f<F;f++)o+=hh[f]*b2f(wd[f*D+d]);
  float s=2*y+rbf(xn[t*D+d]*b2f(dsk[d]));o=rbf(o);float g=rbf(o/(1.f+expf(-o)));
  out[t*D+d]=b2f(x[t*D+d])+s*g;
}
extern "C" void kernel_launch(void* const* d_in,const int* in_sizes,int n_in,void* d_out,int out_size,void* d_ws,size_t ws_size,hipStream_t stream){
  const u16**in=(const u16**)d_in;float*xn=(float*)d_ws;float2*bu=(float2*)(xn+L*D);float*h=(float*)(bu+L*P);
  k_ln<<<L,64,0,stream>>>(in[0],xn);
  k_bu<<<L,P,0,stream>>>(xn,in[3],in[4],in[1],in[2],in[7],bu);
  k_scan<<<7,64,0,stream>>>(bu,in[1],in[2],in[7]);
  k_h<<<L,D,0,stream>>>(xn,in[10],in[11],h);
  k_out<<<L,D,0,stream>>>(in[0],xn,bu,h,in[5],in[6],in[12],in[8],(float*)d_out);
}
```

```cpp
#include <hip/hip_runtime.h>
#include <type_traits>

namespace {

typedef unsigned short u16;
typedef unsigned u32;
typedef unsigned long long u64;
typedef short bf16x8 __attribute__((ext_vector_type(8)));
typedef short s16x4 __attribute__((ext_vector_type(4)));
typedef __bf16 bf16x2 __attribute__((ext_vector_type(2)));
typedef float f32x16 __attribute__((ext_vector_type(16)));
typedef float f32x4 __attribute__((ext_vector_type(4)));
typedef float f32x2 __attribute__((ext_vector_type(2)));
typedef u32 v4u __attribute__((ext_vector_type(4)));
typedef u32 v2u __attribute__((ext_vector_type(2)));
typedef __amdgpu_buffer_rsrc_t rsrc_t;
#define LAS __attribute__((address_space(3)))
#define GAS __attribute__((address_space(1)))
#define CAS __attribute__((address_space(4)))
#define DI __device__ __forceinline__

constexpr int SEQ = 2048, DM = 448, FF = 1792;
constexpr int NCOMP = 224;

constexpr u32 slot1(int c) { return (u32)(c & 3) * 28672u; }
constexpr u32 L_AUX = 114688, L_MISC = 147456;
constexpr u32 M_STAT = L_MISC, M_CS = L_MISC + 1024, M_ST2 = L_MISC + 3072, M_DUMMY = L_MISC + 3584,
              M_ZERO = L_MISC + 4096, M_SUB = L_MISC + 6144, M_SUMX = L_MISC + 8192, M_SUMQ = L_MISC + 8704,
              M_CSP = L_MISC + 9216, M_EPOCH = L_MISC + 3968, M_X = L_MISC + 8192;
constexpr u32 M_TR = M_X;
constexpr u32 slotA(int k) { return (u32)((k & 3) == 0 ? 6 : ((k & 3) - 1) * 2) * 16384u; }
constexpr u32 slotB(int k) { return (u32)((k % 5) < 2 ? 7 + (k % 5) : ((k % 5) - 2) * 2 + 1) * 16384u; }

constexpr u32 W_FLAG = 4096, W_SG = 348160, W_ST = 462848, W_CS = 495616, W_H = 557056;
constexpr u32 FLAG_STAGE = 64 * 14 * 128;
constexpr u32 W_END = W_H + 32u * 21u * 16384u;
constexpr u32 W_DIAG = 16u << 20;
constexpr u32 OOB = 0x40000000u;
constexpr u32 SPIN_CAP = 1u << 16;
constexpr int CODE_LINES = 300;

constexpr int DRAIN_A = 156;
constexpr int DRAIN_B = 20;
#define HPRIO 0

struct Params {
  const u16 *x, *lre, *lim, *bre, *bim, *cre, *cim, *ldt, *dsk, *wg, *wu, *wd;
  float* out;
  char* ws;
};

#define IC(v) std::integral_constant<int, (v)>{}
template <int B, int E, class F> DI void sfor(F&& f) {
  if constexpr (B < E) { f(std::integral_constant<int, B>{}); sfor<B + 1, E>(f); }
}
DI float asf(u32 v) { return __builtin_bit_cast(float, v); }
DI u32 asu(float v) { return __builtin_bit_cast(u32, v); }
DI float bf2f(u32 v) { return asf(v << 16); }
DI u32 pk2(float a, float b) { bf16x2 c; c[0] = (__bf16)a; c[1] = (__bf16)b; return __builtin_bit_cast(u32, c); }
DI float rbf(float a) { return bf2f(pk2(a, a) & 0xffffu); }
DI rsrc_t mk(const void* p, u32 bytes) { return __builtin_amdgcn_make_buffer_rsrc((void*)p, (short)0, (int)bytes, 0x00020000); }
DI f32x16 mfma32(bf16x8 a, bf16x8 b, f32x16 c) { return __builtin_amdgcn_mfma_f32_32x32x16_bf16(a, b, c, 0, 0, 0); }
DI f32x4 mfma16(bf16x8 a, bf16x8 b, f32x4 c) { return __builtin_amdgcn_mfma_f32_16x16x32_bf16(a, b, c, 0, 0, 0); }
DI void pin() { __builtin_amdgcn_sched_barrier(0); }
DI void bar() { asm volatile("s_barrier" ::: "memory"); }
template <int N> DI void wait_vm() { asm volatile("s_waitcnt vmcnt(%0)" ::"n"(N) : "memory"); }
DI void wait_lgkm() { asm volatile("s_waitcnt lgkmcnt(0)" ::: "memory"); }

template <class T> DI T ldsr(LAS char* L, u32 off) { return *(LAS T*)(L + off); }
template <class T> DI void ldsw(LAS char* L, u32 off, T v) { *(LAS T*)(L + off) = v; }
DI bf16x8 ldtr(LAS char* L, u32 off, u32 step) {
  const s16x4 lo = __builtin_amdgcn_ds_read_tr16_b64_v4i16((LAS s16x4*)(L + off));
  const s16x4 hi = __builtin_amdgcn_ds_read_tr16_b64_v4i16((LAS s16x4*)(L + off + step));
  return __builtin_shufflevector(lo, hi, 0, 1, 2, 3, 4, 5, 6, 7);
}
template <u32 LO, u32 SO> DI void dma(u32 lbase, u32 voff, rsrc_t rs, u32 sbase) {
  u32 t;
  asm volatile("s_add_u32 m0, %1, %2\n\ts_add_u32 %0, %3, %4\n\tbuffer_load_dwordx4 %5, %6, %0 offen lds"
               : "=&s"(t) : "s"(lbase), "n"(LO), "s"(sbase), "n"(SO), "v"(voff), "s"(rs) : "memory", "scc");
}
template <u32 LO, u32 SO> DI u32 dma_set(u32 lbase, u32 sbase) {
  u32 t;
  asm volatile("s_add_u32 m0, %1, %2\n\ts_add_u32 %0, %3, %4" : "=&s"(t) : "s"(lbase), "n"(LO), "s"(sbase), "n"(SO) : "memory", "scc");
  return t;
}
template <u32 O> DI void dma_go(u32 voff, rsrc_t rs, u32 soff) {
  asm volatile("buffer_load_dwordx4 %0, %1, %2 offen offset:%3 lds" ::"v"(voff), "s"(rs), "s"(soff), "n"(O) : "memory");
}
template <u32 LO, u32 SO> DI void dma4_sc1(u32 lbase, u32 voff, rsrc_t rs, u32 sbase) {
  u32 t;
  asm volatile("s_add_u32 m0, %1, %2\n\ts_add_u32 %0, %3, %4\n\t"
               "buffer_load_dwordx4 %5, %6, %0 offen sc1 lds\n\tbuffer_load_dwordx4 %5, %6, %0 offen offset:1024 sc1 lds\n\t"
               "buffer_load_dwordx4 %5, %6, %0 offen offset:2048 sc1 lds\n\tbuffer_load_dwordx4 %5, %6, %0 offen offset:3072 sc1 lds"
               : "=&s"(t) : "s"(lbase), "n"(LO), "s"(sbase), "n"(SO), "v"(voff), "s"(rs) : "memory", "scc");
}
DI void ald16(u32& d, u32 voff, rsrc_t rs) { asm volatile("buffer_load_ushort %0, %1, %2, 0 offen" : "=v"(d) : "v"(voff), "s"(rs) : "memory"); }
template <u32 O = 0> DI void ald64(v2u& d, u32 voff, rsrc_t rs) { asm volatile("buffer_load_dwordx2 %0, %1, %2, 0 offen offset:%3 sc1" : "=v"(d) : "v"(voff), "s"(rs), "n"(O) : "memory"); }
template <u32 O = 0> DI void ald128(v4u& d, u32 voff, rsrc_t rs) { asm volatile("buffer_load_dwordx4 %0, %1, %2, 0 offen offset:%3 sc1" : "=v"(d) : "v"(voff), "s"(rs), "n"(O) : "memory"); }
DI void ast64(v2u d, u32 voff, rsrc_t rs) { asm volatile("buffer_store_dwordx2 %0, %1, %2, 0 offen sc1" ::"v"(d), "v"(voff), "s"(rs) : "memory"); }
template <u32 O = 0> DI void ast128(v4u d, u32 voff, rsrc_t rs) { asm volatile("buffer_store_dwordx4 %0, %1, %2, 0 offen offset:%3 sc1\n\ts_nop 1" ::"v"(d), "v"(voff), "s"(rs), "n"(O) : "memory"); }
DI float addf(float a, float b) { float d; asm("v_add_f32 %0, %1, %2" : "=v"(d) : "v"(a), "v"(b)); return d; }
DI u32 opaque_s(u32 v) { asm volatile("" : "+s"(v)); return v; }
template <class T> DI void landed(T& v) { asm volatile("" : "+v"(v)); }
DI u32 opaque(u32 v) { asm volatile("" : "+v"(v)); return v; }
DI float pick(float a, float b, u64 m) { float d; asm("v_cndmask_b32 %0, %1, %2, %3" : "=v"(d) : "v"(a), "v"(b), "s"(m)); return d; }

DI bool all(bool c) { return __builtin_amdgcn_ballot_w64(!c) == 0; }

struct cf { float r, i; };
DI void landed(cf& v) { asm volatile("" : "+v"(v.r), "+v"(v.i)); }
DI cf cmul(cf a, cf b) { return {__builtin_fmaf(a.r, b.r, -(a.i * b.i)), __builtin_fmaf(a.r, b.i, a.i * b.r)}; }
DI f32x2 as2(cf v) { landed(v); return f32x2{v.r, v.i}; }
DI cf cfma(cf a, cf b, cf c) { return {__builtin_fmaf(a.r, b.r, __builtin_fmaf(-a.i, b.i, c.r)), __builtin_fmaf(a.r, b.i, __builtin_fmaf(a.i, b.r, c.i))}; }

#define CSTAMP(I)
#define HSTAMP(I)

DI void compute_waves(const Params& p, LAS char* L, const int w, const int lane, const int ti, const int tj, const int rt, const int ct) {
  const u32 lb = (u32)(size_t)L;
  const rsrc_t rs_x = mk(p.x, SEQ * DM * 2), rs_bre = mk(p.bre, DM * DM * 2), rs_bim = mk(p.bim, DM * DM * 2);
  const u32 vx = (8 * w + (lane >> 3)) * 896 + (((lane & 7) ^ ((4 * w + (lane >> 4)) & 7)) << 4);
  const u32 l1 = opaque_s(lb + w * 1024), sx = opaque_s(ti * 128 * 896), sb = opaque_s(tj * 32 * 896);
  auto load_ssm = [&](auto C) {
    constexpr int c = C;
    sfor<0, 4>([&](auto N) { dma<slot1(c) + 4096 * N, 32 * N * 896 + c * 128>(l1, vx, rs_x, sx); });
    dma<slot1(c) + 16384, c * 128>(l1, vx, rs_bre, sb);
    dma<slot1(c) + 20480, c * 128>(l1, vx, rs_bim, sb);
  };
  asm volatile("s_nop 4" ::: "memory");
  load_ssm(IC(0));
  load_ssm(IC(1));
  constexpr int NTOUCH = 0;
  pin();

  const int r = lane & 31, h = lane >> 5;
  const int tau = 16 * ((r >> 2) & 1) + (r & 3) + 4 * (r >> 3);
  const rsrc_t rs_ws = mk(p.ws, W_END), rs_w1 = mk(w >= 2 ? p.wu : p.wg, DM * FF * 2);
  const u32 vw = (lane >> 3) * 3584 + (((lane & 7) ^ ((((lane >> 4) & 1) | (w & 2)) << 1)) << 4);
  const u32 l7 = opaque_s(lb + w * 7168), sw = opaque_s(56 * (w & 1) * 3584 + 128 * tj);
  u32 ax[4], aw[4];
  {
    const int xrow = 32 * w + tau, g = lane >> 4, q = (lane >> 2) & 3, pp = lane & 3;
    const int gsw = ((q >> 1) & 1) | ((pp >> 1) << 1);
    sfor<0, 4>([&](auto S) {
      constexpr int s = S;
      ax[s] = xrow * 128 + (((2 * s + h) ^ ((xrow >> 1) & 7)) << 4);
      aw[s] = opaque((pp >> 1) * 14336 + (8 * (g >> 1) + q) * 128 + (2 * (s ^ gsw) + (pp & 1)) * 16 + 8 * (g & 1));
    });
  }
  const f32x16 zero16 = {0, 0, 0, 0, 0, 0, 0, 0, 0, 0, 0, 0, 0, 0, 0, 0};
  bf16x8 xf[28], wf[6];
  f32x16 acc[2][4];
  const u32 hbase = opaque_s(W_H + ((2 * ti + (w >> 1)) * 21 + (tj >> 1)) * 16384 + ((w & 1) * 8 + 4 * (tj & 1)) * 1024);
  const u32 a_m = opaque(M_STAT + w * 256 + tau * 4);
  const u32 vfs = opaque(lane == 0 ? W_FLAG + ((4 * ti + w) * 14 + tj) * 128 : OOB);
  v2u flagv;

  u32 wbase[2];
  bf16x8 mfrag;
  const u32 a_cs = opaque(M_CS + r * 16);
  auto ffn_read = [&](auto N) {
    constexpr int n = N, y = n % 116, cc = y < 84 ? y / 28 : 3, c = 7 + 4 * (n / 116) + cc;
    constexpr int T = y < 84 ? (y % 28) / 7 : (y - 84) / 8, kk = y < 84 ? y % 7 : (y - 84) % 8;
    if constexpr (kk == 7) {
      wf[n % 6] = ldsr<bf16x8>(L, a_cs + T * 512);
    } else if constexpr (c & 2) {
      if constexpr (kk <= 1 && (n & 1)) wbase[T & 1] = opaque(aw[T] + slot1(c));
      wf[n % 6] = ldtr(L, wbase[T & 1] + 2048 * kk, 512);
    } else {
      wf[n % 6] = ldtr(L, aw[T] + slot1(c) + 2048 * kk, 512);
    }
  };
  wait_vm<6 + NTOUCH>();
  bar();
  sfor<0, 7>([&](auto C) {
    constexpr int c = C;
    CSTAMP(c);
    sfor<0, 4>([&](auto S) { xf[4 * c + S] = ldsr<bf16x8>(L, ax[S] + slot1(c)); });
    if constexpr (c + 2 < 7) load_ssm(IC(c + 2));
    else sfor<0, 7>([&](auto N) { dma<slot1(c + 2) + 1024 * N, (112 * (c - 5) + 8 * N) * 3584>(l7, vw, rs_w1, sw); });
    wait_vm<(c + 2 < 7 ? 6 : 7) + (c == 0 ? NTOUCH : 0)>();
    wait_lgkm();
    bar();
  });
  ffn_read(IC(1)); ffn_read(IC(0)); ffn_read(IC(3)); ffn_read(IC(2));

  float hv[8];
  v4u hp;
  auto epi_op = [&](auto U, auto O) {
    constexpr int u = U, o = O;
    if constexpr (o >= 0 && o < 56) {
      constexpr int T = o / 14, y = o % 14;
      constexpr int e = y < 3 ? y : y == 4 || y == 5 ? y - 1 : y == 7 || y == 8 ? y - 2 : 7;
      if constexpr (y == 3 || y == 6 || y == 9) hp[y / 3 - 1] = opaque(pk2(hv[2 * (y / 3 - 1)], hv[2 * (y / 3 - 1) + 1]));
      else if constexpr (y == 12) hp[3] = opaque(pk2(hv[6], hv[7]));
      else if constexpr (y == 13) __builtin_amdgcn_raw_buffer_store_b128(hp, rs_ws, lane * 16, hbase + (u * 7 * 16 + T) * 1024, 16);
      else if constexpr (y != 11) { hv[e] = acc[u][T][8 * (e >> 2) + (e & 3)] * acc[u][T][8 * (e >> 2) + 4 + (e & 3)]; landed(hv[e]); }
    }
  };

  const int rb = w >> 1, cb = w & 1;
  u32 a2, bw, bc[4];
  bf16x8 af[6], bf[6];
  u32 pa, pb, pc4[4];
  auto p2_addr = [&]() {
    const int ln = opaque(lane);
    const int g = ln >> 4, q = (ln >> 2) & 3, pp = ln & 3, row = 32 * cb + (ln & 31), h = ln >> 5;
    a2 = rb * 8192 + ln * 16;
    bw = (8 * (g >> 1) + q) * 128 + (((4 * cb + 2 * (g & 1) + (pp >> 1)) ^ (((q >> 1) & 1) << 2)) << 4) + 8 * (pp & 1);
    sfor<0, 4>([&](auto S) { constexpr int s = S; bc[s] = row * 128 + (((2 * s + h) ^ ((row >> 1) & 7)) << 4); });
  };
  auto p2_read = [&](auto M) {
    constexpr int m = M, k = m >> 3, ks = m & 7;
    if constexpr (ks == 0) pa = opaque(a2 + slotA(k));
    if constexpr (k < 14) {
      if constexpr (ks == 0) pb = opaque(bw + slotB(k));
      bf[m % 6] = ldtr(L, pb + 2048 * ks, 512);
    } else {
      if constexpr (ks < 4) pc4[ks] = opaque(bc[ks] + slotB(k));
      bf[m % 6] = ldsr<bf16x8>(L, pc4[ks & 3] + (ks >> 2) * 8192);
    }
    af[m % 6] = ldsr<bf16x8>(L, pa + ks * 1024);
  };

  constexpr int E0 = 94;
  float mval;
  sfor<0, 232>([&](auto N) {
    constexpr int n = N, u = n / 116, y = n % 116, cc = y < 84 ? y / 28 : 3;
    constexpr int T = y < 84 ? (y % 28) / 7 : (y - 84) / 8, kk = y < 84 ? y % 7 : (y - 84) % 8;
    if constexpr (n + 4 < 232) {
      constexpr int z = (n + 4) % 116;
      if constexpr (z == 0 || z == 28 || z == 56 || z == 84) {
        constexpr int bi = 7 + 4 * ((n + 4) / 116) + z / 28;
        if constexpr (bi == 8) wait_vm<0>();
        bar();
      }
      ffn_read(IC((n & 1) ? n + 3 : n + 5));
    } else {
      if constexpr (n == 228) { bar(); p2_read(IC(0)); }
      p2_read(IC(n - 227));
    }
    if constexpr (kk == 0) CSTAMP(7 + 16 * u + 4 * cc + T);
    pin();
    if constexpr (kk == 7) acc[u][T] = mfma32(wf[n % 6], mfrag, acc[u][T]);
    else acc[u][T] = mfma32(wf[n % 6], xf[7 * cc + kk], (cc | kk) ? acc[u][T] : zero16);
    pin();
    if constexpr (n == 60) mval = ldsr<float>(L, a_m);
    if constexpr (n == DRAIN_A - 6) flagv = ldsr<v2u>(L, M_EPOCH);
    if constexpr (n == DRAIN_A) { wait_vm<0>(); ast64(flagv, vfs, rs_ws); }
    if constexpr (n == 200) p2_addr();
    if constexpr (n == 66) {
      const u32 mb = h ? 0u : (asu(mval) >> 16) ^ 0x8000u;
      mfrag = __builtin_bit_cast(bf16x8, v4u{mb * 0x10001u, mb, 0u, 0u});
      landed(mfrag);
    }
    if constexpr (n >= E0) sfor<0, 2>([&](auto J) { epi_op(IC(0), IC(7 * (n - E0) / 4 + J < 7 * (n + 1 - E0) / 4 ? 7 * (n - E0) / 4 + J : -1)); });
    if constexpr (n >= 116 + E0) sfor<0, 2>([&](auto J) { epi_op(IC(1), IC(7 * (n - 116 - E0) / 4 + J < 7 * (n + 1 - 116 - E0) / 4 ? 7 * (n - 116 - E0) / 4 + J : -1)); });
    pin();
  });
  CSTAMP(39);

  f32x16 accF, accY;
  u32 dv;
  float xv[16], r2[16], sk[16];
  f32x4 st;
  const int l2 = opaque(lane);
  const int trow = 64 * rt + 32 * rb + 16 * (l2 >> 5), dcol = 64 * ct + 32 * cb + (l2 & 31);
  const u32 sbase = opaque(M_ST2 + (32 * rb + 16 * (l2 >> 5)) * 8);
  const u32 xbase = opaque(M_X + (32 * rb + 16 * (l2 >> 5)) * 128 + (32 * cb + (l2 & 31)) * 2);
  const rsrc_t rs_out = mk(p.out, SEQ * DM * 4);
  const u32 eoff = trow * DM + dcol;
  sfor<0, 168>([&](auto M) {
    constexpr int m = M, k = m >> 3;
    if constexpr (m + 5 < 168) {
      if constexpr ((m + 5) % 8 == 0) bar();
      p2_read(IC(m + 5));
    }
    if constexpr ((m & 7) == 0) CSTAMP(40 + k);
    pin();
    if constexpr (k < 14) accF = mfma32(af[m % 6], bf[m % 6], m ? accF : zero16);
    else accY = mfma32(af[m % 6], bf[m % 6], m > 112 ? accY : zero16);
    pin();
    if constexpr (m < 6) sfor<0, 3>([&](auto J) { epi_op(IC(1), IC(7 * (116 - E0) / 4 + 3 * m + J)); });
    if constexpr (m == DRAIN_B) {
      wait_vm<0>();
      ast64(flagv, vfs + 2 * FLAG_STAGE, rs_ws);
      dv = ((const GAS u16*)p.dsk)[dcol];
    }
    if constexpr (m >= 40 && m < 104) {
      constexpr int e = (m - 40) / 4, y = (m - 40) % 4;
      if constexpr (y == 0) {
        if constexpr ((e & 1) == 0) st = ldsr<f32x4>(L, sbase + e * 8);
        xv[e] = bf2f(ldsr<u16>(L, xbase + e * 128));
      }
      if constexpr (y == 2) {
        const float mm = st[2 * (e & 1)], rr = st[2 * (e & 1) + 1];
        sk[e] = rbf(rbf(rbf(xv[e] - mm) * rr) * bf2f(dv));
        r2[e] = rr * rr;
        landed(sk[e]); landed(r2[e]);
      }
    }
    if constexpr (m >= 114 && m < 162 && (m - 114) % 3 == 0) {
      constexpr int e = (m - 114) / 3;
      const float o = rbf(accF[e] * r2[e]);
      r2[e] = rbf(o * __builtin_amdgcn_rcpf(1.f + __expf(-o)));
      xv[e] = __builtin_fmaf(sk[e], r2[e], xv[e]);
      landed(r2[e]); landed(xv[e]);
    }
    pin();
  });

  CSTAMP(61);
  sfor<0, 16>([&](auto E) {
    constexpr int e = E;
    __builtin_amdgcn_raw_buffer_store_b32(asu(__builtin_fmaf(accY[e], r2[e], xv[e])), rs_out, eoff * 4, e * DM * 4, 16);
  });
}

DI void helper_waves(const Params& p, LAS char* L, const int w, const int lane, const int ti, const int tj, const int rt, const int ct) {
  const int pc = lane & 31, h = lane >> 5;
  const u32 lb = (u32)(size_t)L;
  const rsrc_t rs_ws = mk(p.ws, W_END);
  u32 plr, pli, pdt;
  v2u ep;
  ald16(plr, (32 * tj + pc) * 2, mk(p.lre, DM * 2));
  ald16(pli, (32 * tj + pc) * 2, mk(p.lim, DM * 2));
  ald16(pdt, (32 * tj + pc) * 2, mk(p.ldt, DM * 2));
#define SYNC(N, I) do { pin(); wait_vm<N>(); wait_lgkm(); bar(); pin(); } while (0)
#define SBAR(I) bar()
  const rsrc_t rs_w1 = mk(w >= 2 ? p.wu : p.wg, DM * FF * 2), rs_wd = mk(p.wd, FF * DM * 2);
  const rsrc_t rs_cre = mk(p.cre, DM * DM * 2), rs_cim = mk(p.cim, DM * DM * 2);
  const u32 vx = (8 * w + (lane >> 3)) * 896 + (((lane & 7) ^ ((4 * w + (lane >> 4)) & 7)) << 4);
  const u32 vw = (lane >> 3) * 3584 + (((lane & 7) ^ ((((lane >> 4) & 1) | (w & 2)) << 1)) << 4);
  const u32 va = lane * 16;
  const u32 vd = (lane >> 3) * 896 + (((lane & 7) ^ (((lane >> 4) & 1) << 2)) << 4);
  const u32 l1 = opaque_s(lb + w * 1024), l7 = opaque_s(lb + w * 7168), l4 = opaque_s(lb + w * 4096);
  const u32 sw = opaque_s(56 * (w & 1) * 3584 + 128 * tj), sa = opaque_s(W_H + rt * 21 * 16384 + w * 4096);
  const u32 sd = opaque_s(32 * w * 896 + ct * 128), sc = opaque_s(64 * ct * 896);
  u32 vw4[4], vd4[4], so;
  sfor<0, 4>([&](auto N) { vw4[N] = opaque(vw + N * (8 * 3584 - 1024)); vd4[N] = opaque(vd + N * (8 * 896 - 1024)); });
  auto ldw = [&](auto C, auto N) {
    constexpr int c = C, n = N, u = (c - 7) / 4, cc = (c - 7) % 4;
    if constexpr ((n & 3) == 0) so = dma_set<slot1(c) + 1024 * n, (112 * cc + 8 * n) * 3584 + u * 1792>(l7, sw);
    dma_go<1024 * (n & 3)>(vw4[n & 3], rs_w1, so);
    pin();
  };
  auto ldd = [&](auto K, auto N) {
    if constexpr (N == 0) so = dma_set<slotB(K), 128 * K * 896>(l4, sd);
    dma_go<1024 * N>(vd4[N], rs_wd, so);
    pin();
  };
  auto loadA = [&](auto K) { dma4_sc1<slotA(K), K * 16384>(l4, va, rs_ws, sa); };
  auto loadB = [&](auto K) {
    constexpr int k = K;
    if constexpr (k < 14) {
      sfor<0, 4>([&](auto N) { ldd(K, N); });
    } else {
      sfor<0, 2>([&](auto N) {
        dma<slotB(k) + 4096 * N, 32 * N * 896 + (k - 14) * 128>(l1, vx, rs_cre, sc);
        dma<slotB(k) + 8192 + 4096 * N, 32 * N * 896 + (k - 14) * 128>(l1, vx, rs_cim, sc);
      });
    }
  };

  const int tau = 16 * ((pc >> 2) & 1) + (pc & 3) + 4 * (pc >> 3);
  u32 ax[4], ab[4];
  sfor<0, 4>([&](auto S) {
    const int s = (S + w) & 3, xrow = 32 * w + tau;
    ax[S] = opaque(xrow * 128 + (((2 * s + h) ^ ((xrow >> 1) & 7)) << 4));
    ab[S] = opaque(pc * 128 + (((2 * s + h) ^ ((pc >> 1) & 7)) << 4) + 16384);
  });
  const short one = ((lane & 15) == ((lane >> 4) & 1)) ? (short)0x3f80 : (short)0;
  bf16x8 sel = {one, one, one, one, one, one, one, one};
  landed(sel);
  const u32 a_csp = opaque(M_CSP + pc * 4), a_sub = opaque(M_SUB + pc * 8), a_own = opaque(M_SUB + (2 * w + h) * 256 + pc * 8);
  const u32 a_csw = opaque(lane < 16 ? M_CSP + w * 256 + lane * 4 : M_DUMMY);
  const u32 a_sq = opaque(h == ((pc >> 2) & 1) ? M_SUMQ + w * 128 + tau * 4 : M_DUMMY + lane * 4);
  const u32 a_sx = opaque((lane & 15) < 2 ? M_SUMX + w * 128 + (16 * ((lane >> 4) & 1) + 8 * (lane & 1) + 4 * (lane >> 5)) * 4 : M_DUMMY + 256);
  const u32 a_stw = opaque(M_STAT + w * 256 + h * 128 + pc * 4), a_str = opaque(M_STAT + w * 256 + h * 64);
  const int d0 = ti - 1 - 2 * (2 * w + h);
  const u32 vs0 = opaque(d0 >= 0 ? W_SG + (tj * 16 + d0) * 512 + pc * 16 : OOB);
  const u32 vs1 = opaque(d0 >= 1 ? W_SG + (tj * 16 + d0 - 1) * 512 + pc * 16 : OOB);
  const u32 vsw = opaque(w == 3 && h ? W_SG + (tj * 16 + ti) * 512 + pc * 16 : OOB);
  const u32 vst = opaque(tj == 0 && lane < 32 ? W_ST + (ti * 128 + w * 32 + lane) * 16 : OOB);
  const u32 vcs = opaque(W_CS + tj * 2048 + (32 * w + pc) * 16);
  const u32 a_csf = opaque(M_CS + (2 * (w & 1) + (pc >> 4)) * 512 + (16 * ((pc >> 2) & 1) + 4 * ((pc >> 3) & 1) + 8 * (w >> 1) + (pc & 3)) * 16);
  const u32 vfl = opaque(W_FLAG + (rt * 28 + (lane < 28 ? lane : 27)) * 128);
  const u32 vfs = opaque(lane == 0 ? W_FLAG + FLAG_STAGE + ((4 * ti + w) * 14 + tj) * 128 : OOB);
  const u32 a_tw = opaque(M_TR + w * 2048 + pc * 64 + (((2 * h) ^ ((pc >> 1) & 3)) << 4));
  const u32 a_tr = opaque(M_TR + w * 2048 + (8 * h + ((lane >> 2) & 3)) * 64 + (((2 * (lane & 1) + ((lane >> 4) & 1)) ^ ((lane >> 3) & 1)) << 4) + 8 * ((lane >> 1) & 1));
  const u32 vhs = opaque(W_H + ((2 * ti + (w >> 1)) * 21 + 14 + (tj >> 1)) * 16384 + ((w & 1) * 8 + 2 * (tj & 1)) * 1024 + va);
  ldsw<f32x2>(L, a_own - 2048, f32x2{0.f, 0.f});
  cf lam, lam4, lam16, lam128, lamk, lamp, coef, l32, l64, l256, l512, c0, c1;
  float lr, li, dt, t0, t1, t2, t3, th, kq;
  int iq;
  v2u flagv;
  const cf unit = {1.f, 0.f};
  constexpr float L2E = 1.44269502e+00f, L2E_LO = 1.92596299e-08f, LN2 = 6.93147182e-01f;
  auto sq = [&](cf a) { return cf{__builtin_fmaf(a.r, a.r, -(a.i * a.i)), (a.r + a.r) * a.i}; };
  auto lam_op = [&](auto O) {
    constexpr int o = O;
    if constexpr (o == 0) { wait_vm<0>(); landed(plr); landed(pli); landed(pdt); lr = bf2f(plr); li = bf2f(pli); t0 = bf2f(pdt); t1 = t0 * L2E; landed(t1); }
    if constexpr (o == 1 || o == 4) { t2 = __builtin_fmaf(t0, L2E_LO, __builtin_fmaf(t0, L2E, -t1)) * LN2; t1 = __builtin_amdgcn_exp2f(t1); landed(t1); landed(t2); }
    if constexpr (o == 2) { dt = rbf(__builtin_fmaf(t1, t2, t1)); landed(dt); ald64(ep, opaque(0), rs_ws); }
    if constexpr (o == 3) { t0 = dt * lr; th = dt * li; t1 = t0 * L2E; kq = __builtin_rintf(th * 0.636619772f); landed(t1); landed(kq); }
    if constexpr (o == 5) {
      dt = __builtin_fmaf(t1, t2, t1);
      th = __builtin_fmaf(kq, -5.39030253e-15f, __builtin_fmaf(kq, -3.13916473e-07f, __builtin_fmaf(kq, -1.57079601e+00f, th)));
      landed(dt); landed(th);
    }
    if constexpr (o == 6) { t0 = th * th; t1 = __builtin_fmaf(t0, __builtin_fmaf(t0, __builtin_fmaf(t0, 2.86567956e-6f, -1.98559923e-4f), 8.33338592e-3f), -1.66666672e-1f); landed(t1); }
    if constexpr (o == 7) { t1 = __builtin_fmaf(th * t0, t1, th); t2 = __builtin_fmaf(t0, __builtin_fmaf(t0, 2.44677067e-5f, -1.38877297e-3f), 4.16666567e-2f); landed(t1); landed(t2); }
    if constexpr (o == 8) { t2 = __builtin_fmaf(t0, __builtin_fmaf(t0, t2, -0.5f), 1.f); iq = (int)kq; t3 = __builtin_fmaf(lr, lr, li * li); landed(t2); landed(iq); landed(t3); }
    if constexpr (o == 9) { const bool odd = iq & 1; t0 = odd ? t2 : t1; t1 = odd ? t1 : t2; landed(t0); landed(t1); }
    if constexpr (o == 10) { t0 = asf(asu(t0) ^ ((u32)(iq & 2) << 30)); t3 = __builtin_amdgcn_rcpf(t3); landed(t0); landed(t3); }
    if constexpr (o == 11) { t1 = asf(asu(t1) ^ ((u32)((iq + 1) & 2) << 30)); landed(t1); }
    if constexpr (o == 12) { lam = cf{dt * t1, dt * t0}; t3 += t3; t0 = lam.r - 1.f; landed(lam); landed(t3); landed(t0); }
    if constexpr (o == 13) { coef = cf{__builtin_fmaf(t0, lr, lam.i * li), __builtin_fmaf(lam.i, lr, -(t0 * li))}; landed(coef); }
    if constexpr (o == 14) { coef = cf{coef.r * t3, coef.i * t3}; landed(coef); }
    if constexpr (o == 15) { lam4 = sq(lam); landed(lam4); }
    if constexpr (o == 16) { lam4 = sq(lam4); landed(lam4); }
    if constexpr (o == 17) { lam16 = sq(lam4); landed(lam16); }
    if constexpr (o == 18) { lam16 = sq(lam16); landed(lam16); }
    if constexpr (o == 19) { l32 = sq(lam16); landed(l32); }
    if constexpr (o == 20) { l64 = sq(l32); landed(l64); }
    if constexpr (o == 21) { lam128 = sq(l64); landed(lam128); }
    if constexpr (o == 22) { l256 = sq(lam128); landed(l256); }
    if constexpr (o == 23) { l512 = sq(l256); landed(l512); }
    if constexpr (o == 24) { c0 = h ? lam16 : unit; c1 = (w & 1) ? l32 : unit; landed(c0); landed(c1); }
    if constexpr (o == 25) { lamk = cmul(c0, c1); landed(lamk); }
    if constexpr (o == 26) { c0 = (w & 2) ? l64 : unit; c1 = h ? l256 : unit; landed(c0); landed(c1); }
    if constexpr (o == 27) { lamk = cmul(lamk, c0); landed(lamk); }
    if constexpr (o == 28) { lamk = cmul(lamk, coef); landed(lamk); }
    if constexpr (o == 29) { c0 = (w & 1) ? l512 : unit; landed(c0); }
    if constexpr (o == 30) { lamp = cmul(c0, c1); landed(lamp); }
    if constexpr (o == 31) { c0 = sq(l512); landed(c0); }
    if constexpr (o == 32) { c0 = (w & 2) ? c0 : unit; landed(c0); }
    if constexpr (o == 33) { lamp = cmul(lamp, c0); landed(lamp); }
    if constexpr (o == 34) {
      wait_vm<0>();
      landed(ep);
      const u32 lo = ep[0] + 1;
      flagv = v2u{lo, ep[1] + (lo == 0)};
      ldsw<v2u>(L, M_EPOCH, flagv);
    }
  };
  const f32x16 zero16 = {0, 0, 0, 0, 0, 0, 0, 0, 0, 0, 0, 0, 0, 0, 0, 0};
  const f32x4 zero4 = {0, 0, 0, 0};
  bf16x8 xf[3], bre[3], bim[3];
  f32x16 are, aim, gram;
  f32x4 sum, csr, csi;
  u32 xa, ba;
  auto ssm_read = [&](auto K, auto J) {
    constexpr int ks = K, c = ks >> 2, s = ks & 3;
    if constexpr (ks < 28) {
      if constexpr (J == 0) { ba = (c & 2) ? opaque(ab[s] + slot1(c)) : ab[s] + slot1(c); bim[ks % 3] = ldsr<bf16x8>(L, ba + 4096); }
      if constexpr (J == 1) bre[ks % 3] = ldsr<bf16x8>(L, ba);
      if constexpr (J == 2) { xa = (c & 2) ? opaque(ax[s] + slot1(c)) : ax[s] + slot1(c); xf[ks % 3] = ldsr<bf16x8>(L, xa); }
    }
  };
  wait_lgkm();
  bar();
  sfor<0, 6>([&](auto J) { ssm_read(IC(J / 3), IC(J % 3)); });
  sfor<0, 28>([&](auto K) {
    constexpr int ks = K, o = 3 * (ks - 3);
    pin();
    gram = mfma32(xf[ks % 3], xf[ks % 3], ks ? gram : zero16);
    pin();
    if constexpr (((ks + 2) & 3) == 0) {
      if constexpr (ks == 26) wait_lgkm();
      bar();
    }
    ssm_read(IC(ks + 2), IC(0));
    lam_op(IC(o));
    if constexpr (ks == 25) { ldsw<float>(L, a_csw, csr[0]); ldsw<float>(L, a_csw + 64, csr[1]); }
    pin();
    are = mfma32(xf[ks % 3], bre[ks % 3], ks ? are : zero16);
    pin();
    ssm_read(IC(ks + 2), IC(1));
    lam_op(IC(o + 1));
    if constexpr (ks == 25) { ldsw<float>(L, a_csw + 128, csi[0]); ldsw<float>(L, a_csw + 192, csi[1]); }
    pin();
    aim = mfma32(xf[ks % 3], bim[ks % 3], ks ? aim : zero16);
    pin();
    ssm_read(IC(ks + 2), IC(2));
    lam_op(IC(o + 2));
    pin();
    sum = mfma16(xf[ks % 3], sel, ks ? sum : zero4);
    if constexpr ((ks & 3) == 0) {
      csr = mfma16(sel, bre[ks % 3], ks ? csr : zero4);
      csi = mfma16(sel, bim[ks % 3], ks ? csi : zero4);
    }
    pin();
  });
  const u32 tag = flagv[0];

  v4u cg;
  auto cs_issue = [&](int u) { ald128(cg, vcs + u * 28672, rs_ws); };
  auto cs_finish = [&](int u) {
    landed(cg);
#pragma unroll 1
    for (u32 n = 0; n < SPIN_CAP && !all(cg[1] == tag && cg[3] == tag); ++n) {
      __builtin_amdgcn_s_sleep(2);
      cs_issue(u); wait_vm<0>(); landed(cg);
    }
    ldsw<v4u>(L, a_csf, v4u{cg[0], cg[2], 0u, 0u});
  };
  v2u fl;
  auto flag_issue = [&](int stage) { ald64(fl, vfl + stage * FLAG_STAGE, rs_ws); };
  auto flag_finish = [&](int stage) {
    landed(fl);
#pragma unroll 1
    for (u32 n = 0; n < SPIN_CAP && !all(fl[0] == flagv[0] && fl[1] == flagv[1]); ++n) {
      __builtin_amdgcn_s_sleep(2);
      flag_issue(stage); wait_vm<0>(); landed(fl);
    }
  };

  __builtin_amdgcn_s_setprio(HPRIO);
  cf b[4][4];
  f32x4 mm[4], rr[4];
  f32x2 stat;
  cf cc;
  {
    float t[8], gd[8];
    sfor<0, 8>([&](auto N) { t[N] = ldsr<float>(L, a_csp + N * 128); });
    ldw(IC(9), IC(0));
    cc = cf{(t[0] + t[2]) + (t[4] + t[6]), (t[1] + t[3]) + (t[5] + t[7])};
    landed(cc);
    ldsw<f32x4>(L, a_sx, sum);
    ldw(IC(9), IC(1));
    asm volatile("s_nop 7\n\ts_nop 3" : "+v"(gram));
    sfor<0, 8>([&](auto N) { constexpr int n = N; gd[n] = pick(gram[2 * n], gram[2 * n + 1], 0xaaaaaaaaaaaaaaaaull); });
    sfor<0, 4>([&](auto N) { gd[N] = pick(gd[2 * N], gd[2 * N + 1], 0xccccccccccccccccull); });
    sfor<0, 2>([&](auto N) { gd[N] = pick(gd[2 * N], gd[2 * N + 1], 0xff00ff00ff00ff00ull); });
    ldsw<float>(L, a_sq, pick(gd[0], gd[1], 0xffff0000ffff0000ull));
    const float sx = ldsr<float>(L, a_csp + (M_SUMX - M_CSP) + w * 128), sq = ldsr<float>(L, a_csp + (M_SUMQ - M_CSP) + w * 128);
    ldw(IC(9), IC(2));
    float mean = sx * (1.f / DM);
    landed(mean);
    const float var = fmaxf(__builtin_fmaf(sq, 1.f / DM, -(mean * mean)), 0.f);
    stat = f32x2{rbf(mean), __builtin_amdgcn_rcpf(rbf(__builtin_amdgcn_sqrtf(rbf(rbf(var) + 1e-5f))))};
    ldsw<float>(L, a_stw, h ? stat[1] : stat[0]);
    sfor<0, 4>([&](auto G) { mm[G] = ldsr<f32x4>(L, a_str + G * 16); rr[G] = ldsr<f32x4>(L, a_str + 128 + G * 16); });
    sfor<3, 7>([&](auto N) { ldw(IC(9), N); });
  }
  SYNC(7, 8);

  sfor<0, 4>([&](auto G) {
    constexpr int g = G;
    ldw(IC(10), IC(2 * g));
    sfor<0, 4>([&](auto E) {
      constexpr int e = E;
      b[g][e] = cf{__builtin_fmaf(-mm[g][e], cc.r, are[4 * g + e]), __builtin_fmaf(-mm[g][e], cc.i, aim[4 * g + e])};
    });
    if constexpr (g < 3) ldw(IC(10), IC(2 * g + 1));
    sfor<0, 4>([&](auto E) { constexpr int e = E; b[g][e] = cf{b[g][e].r * rr[g][e], b[g][e].i * rr[g][e]}; landed(b[g][e]); });
  });
  SYNC(7, 9);

  cs_issue(0);
  cf en[4], fin;
  sfor<0, 4>([&](auto G) { en[G] = b[G][0]; });
  sfor<1, 4>([&](auto E) {
    constexpr int e = E;
    ldw(IC(11), IC(2 * e - 2));
    sfor<0, 2>([&](auto G) { en[G] = cfma(lam, en[G], b[G][e]); });
    ldw(IC(11), IC(2 * e - 1));
    sfor<2, 4>([&](auto G) { en[G] = cfma(lam, en[G], b[G][e]); });
  });
  ldw(IC(11), IC(6));
  fin = cfma(lam4, cfma(lam4, cfma(lam4, en[0], en[1]), en[2]), en[3]);
  ldsw<f32x2>(L, a_own, as2(fin));
  wait_vm<7>();
  cs_finish(0);
  SYNC(7, 10);

  cf pre;
  {
    f32x2 v[7];
    sfor<0, 7>([&](auto K) { v[K] = ldsr<f32x2>(L, a_own - (7 - K) * 256); });
    ldw(IC(12), IC(0));
    pre = cf{v[0][0], v[0][1]};
    sfor<1, 7>([&](auto K) {
      pre = cfma(lam16, pre, cf{v[K][0], v[K][1]});
      if constexpr (K & 1) ldw(IC(12), IC(1 + K / 2));
    });
    fin = cfma(lam16, pre, fin);
    landed(fin);
    ast128(v4u{asu(fin.r), tag, asu(fin.i), tag}, vsw, rs_ws);
    ast128(v4u{asu(stat[0]), tag, asu(stat[1]), tag}, vst, rs_ws);
    sfor<4, 7>([&](auto N) { ldw(IC(12), N); });
    pre = cmul(coef, pre);
    sfor<0, 3>([&](auto G) { en[G] = cmul(coef, en[G]); landed(en[G]); });
    landed(pre);
  }
  SYNC(9, 11);

  sfor<0, 4>([&](auto G) {
    ldw(IC(13), IC(2 * G));
    sfor<0, 2>([&](auto E) { b[G][E] = cmul(coef, b[G][E]); });
    if constexpr (G < 3) ldw(IC(13), IC(2 * G + 1));
    sfor<2, 4>([&](auto E) { b[G][E] = cmul(coef, b[G][E]); });
    sfor<0, 4>([&](auto E) { landed(b[G][E]); });
  });
  SYNC(7, 12);

  v4u s0, s1;
  ald128(s0, vs0, rs_ws); ald128(s1, vs1, rs_ws);
  cs_issue(1);
  sfor<0, 7>([&](auto N) { ldw(IC(14), N); });
  wait_vm<7>();
  landed(s0); landed(s1);
  HSTAMP(80);
#pragma unroll 1
  for (u32 n = 0; n < SPIN_CAP && !all((d0 < 0 || (s0[1] == tag && s0[3] == tag)) && (d0 < 1 || (s1[1] == tag && s1[3] == tag))); ++n) {
    __builtin_amdgcn_s_sleep(2);
    ald128(s0, vs0, rs_ws); ald128(s1, vs1, rs_ws); wait_vm<0>(); landed(s0); landed(s1);
  }
  HSTAMP(81);
  {
    const cf t = cmul(lamp, cfma(lam128, cf{asf(s1[0]), asf(s1[2])}, cf{asf(s0[0]), asf(s0[2])}));
    ldsw<f32x2>(L, a_own, as2(t));
  }
  cs_finish(1);
  SYNC(7, 13);

  HSTAMP(82);
  flag_issue(0);
  u32 pr[8], pi[8];
  cf s2[4];
  {
    f32x2 v[8];
    sfor<0, 8>([&](auto K) { v[K] = ldsr<f32x2>(L, a_sub + K * 256); });
    cf t4[4], t;
    sfor<0, 4>([&](auto K) { t4[K] = cf{addf(v[2 * K][0], v[2 * K + 1][0]), addf(v[2 * K][1], v[2 * K + 1][1])}; });
    sfor<0, 2>([&](auto K) { t4[K] = cf{addf(t4[2 * K].r, t4[2 * K + 1].r), addf(t4[2 * K].i, t4[2 * K + 1].i)}; });
    t = cf{addf(t4[0].r, t4[1].r), addf(t4[0].i, t4[1].i)};
    s2[0] = cfma(lamk, t, pre);
    sfor<1, 4>([&](auto G) { s2[G] = cfma(lam4, s2[G - 1], en[G - 1]); });
    landed(s2[3]);
  }
  HSTAMP(83);
  wait_vm<0>();
  HSTAMP(84);
  flag_finish(0);
  HSTAMP(85);
  loadA(IC(0));
  loadB(IC(0));
  HSTAMP(86);
  SYNC(8, 14);
  HSTAMP(87);

  auto pass2 = [&](auto J) {
    constexpr int j = J;
    cf o[4];
    sfor<0, 4>([&](auto G) { o[G] = cfma(lam, s2[G], b[G][2 * j]); });
    sfor<0, 4>([&](auto G) {
      s2[G] = cfma(lam, o[G], b[G][2 * j + 1]);
      landed(o[G]); landed(s2[G]);
      pr[2 * G + j] = opaque(pk2(o[G].r, s2[G].r));
      pi[2 * G + j] = opaque(pk2(-o[G].i, -s2[G].i));
    });
  };
  auto hs_out = [&](const u32* v, u32 voff) {
    ldsw<v4u>(L, a_tw, v4u{v[0], v[1], v[2], v[3]});
    ldsw<v4u>(L, a_tw ^ 16, v4u{v[4], v[5], v[6], v[7]});
    bf16x8 f[2];
    sfor<0, 2>([&](auto Z) {
      const s16x4 lo = __builtin_amdgcn_ds_read_tr16_b64_v4i16((LAS s16x4*)(L + a_tr + Z * 1024));
      const s16x4 hi = __builtin_amdgcn_ds_read_tr16_b64_v4i16((LAS s16x4*)(L + (a_tr ^ 32) + 256 + Z * 1024));
      f[Z] = __builtin_shufflevector(lo, hi, 0, 1, 2, 3, 4, 5, 6, 7);
    });
    ast128<0>(__builtin_bit_cast(v4u, f[0]), voff, rs_ws);
    ast128<1024>(__builtin_bit_cast(v4u, f[1]), voff, rs_ws);
  };
  loadA(IC(1));
  loadB(IC(1));
  pass2(IC(0));
  SYNC(8, 15);
  __builtin_amdgcn_s_setprio(0);

  loadB(IC(2)); loadA(IC(2));
  pass2(IC(1));
  SYNC(8, 16);
  loadA(IC(3)); loadB(IC(3)); loadB(IC(4));
  hs_out(pr, vhs);
  SYNC(14, 17);
  loadA(IC(4)); loadB(IC(5));
  hs_out(pi, vhs + 4096);
  SYNC(16, 18);
  v4u st2;
  const u32 vst2 = w == 2 ? W_ST + (rt * 64 + lane) * 16 : OOB;
  loadA(IC(5)); loadB(IC(6));
  {
    const u32 vxt = (8 * w + (lane >> 3)) * 896 + (lane & 7) * 16, sxt = rt * 64 * 896 + ct * 128;
    dma<M_X, 0>(l1, vxt, mk(p.x, SEQ * DM * 2), sxt);
    dma<M_X + 4096, 32 * 896>(l1, vxt, mk(p.x, SEQ * DM * 2), sxt);
  }
  ald128(st2, vst2, rs_ws);
  SYNC(17, 19);
  sfor<4, 20>([&](auto KK) {
    constexpr int k = KK;
    if constexpr (k == 4 || k == 11) { if (w == 0) flag_issue(k == 4 ? 2 : 1); }
    if constexpr (k + 2 < 21) loadA(IC(k + 2));
    if constexpr (k + 3 < 21) loadB(IC(k + 3));
    if constexpr (k == 5) ast64(flagv, vfs, rs_ws);
    constexpr int cnt = (k + 2 < 21 ? 4 : 0) + (k + 3 < 21 ? 4 : 0) + (k == 5 ? 1 : k == 6 ? 5 : 0);
    if constexpr (k == 4 || k == 11) { wait_vm<cnt>(); if (w == 0) flag_finish(k == 4 ? 2 : 1); }
    if constexpr (k == 4) {
      landed(st2);
      if (w == 2) {
#pragma unroll 1
        for (u32 n = 0; n < SPIN_CAP && !all(st2[1] == tag && st2[3] == tag); ++n) {
          __builtin_amdgcn_s_sleep(2);
          ald128(st2, vst2, rs_ws); wait_vm<0>(); landed(st2);
        }
        ldsw<v2u>(L, M_ST2 + lane * 8, v2u{st2[0], st2[2]});
      }
    }
    SYNC(cnt, k + 16);
  });
}

DI void support_group(const Params& p, LAS char* L, const int s, const u64 code) {
  const int tid = threadIdx.x, lane = tid & 63, w = tid >> 6;
  u32 sink = 0;
  auto touch = [&](const GAS char* a) { asm volatile("global_load_dword %0, %1, off" : "+v"(sink) : "v"(a) : "memory"); };
  if (tid < CODE_LINES / 4) touch((const GAS char*)code + ((s >> 3) + 4 * tid) * 128);
  const u64 epoch = *(const CAS u64*)p.ws + 1;
  const u32 tag = (u32)epoch;
  if (s < 28) {
    const int j = 7 * (s & 1) + (s >> 1) % 7, mat = (s >> 1) / 7;
    const GAS char* W = (const GAS char*)(mat ? p.wu : p.wg) + (56 * w + (lane >> 3)) * 3584 + j * 128 + (lane & 7) * 16;
    v4u v[2][7];
    sfor<0, 14>([&](auto N) { v[N / 7][N % 7] = *(const GAS v4u*)(W + (N / 7) * 1792 + (N % 7) * 8 * 3584); });
    pin();
    sfor<0, 2>([&](auto U) {
      constexpr int u = U;
      float a[8];
      sfor<0, 4>([&](auto E) {
        constexpr int e = E;
        float lo = 0.f, hi = 0.f;
        sfor<0, 7>([&](auto N) { lo += bf2f(v[u][N][e]); hi += asf(v[u][N][e] & 0xffff0000u); });
        a[2 * e] = lo; a[2 * e + 1] = hi;
      });
      sfor<0, 8>([&](auto E) {
        a[E] += __shfl_xor(a[E], 8); a[E] += __shfl_xor(a[E], 16); a[E] += __shfl_xor(a[E], 32);
      });
      if (lane < 8) {
        ldsw<f32x4>(L, u * 2048 + w * 256 + lane * 32, f32x4{a[0], a[1], a[2], a[3]});
        ldsw<f32x4>(L, u * 2048 + w * 256 + lane * 32 + 16, f32x4{a[4], a[5], a[6], a[7]});
      }
      wait_lgkm();
      bar();
      if (w == 0) {
        float t = 0.f;
        sfor<0, 8>([&](auto N) { t += ldsr<float>(L, u * 2048 + N * 256 + lane * 4); });
        const float t0 = rbf(t), t1 = rbf(t - t0);
        __builtin_amdgcn_raw_buffer_store_b128(v4u{pk2(t0, t1), tag, pk2(t - t0 - t1, 0.f), tag}, mk(p.ws, W_END),
                                               W_CS + (u * 14 + j) * 2048 + mat * 1024 + lane * 16, 0, 16);
      }
    });
  }
  sfor<0, 2>([&](auto N) {
    const int o = N * 512 + tid;
    if (o < 588) {
      const int ln = s * 588 + o;
      touch(ln < 12544 ? (const GAS char*)p.wd + ln * 128 : ln < 15680 ? (const GAS char*)p.cre + (ln - 12544) * 128 : (const GAS char*)p.cim + (ln - 15680) * 128);
    }
  });
  if (lane < 28) {
    const int ln = (s >> 3) * 224 + w * 28 + lane;
    sfor<0, 21>([&](auto K) {
      constexpr int k = K;
      if constexpr (k < 14) touch((const GAS char*)p.wd + (k * 896 + ln) * 128);
      else touch((const GAS char*)(ln < 448 ? p.cre : p.cim) + (ln < 448 ? ln : ln - 448) * 896 + (k - 14) * 128);
    });
  }
  asm volatile("s_waitcnt vmcnt(0)" : "+v"(sink)::"memory");
  if (s == 31 && w == 0) {
#pragma unroll 1
    for (u32 n = 0; n < SPIN_CAP; ++n) {
      bool ok = true;
      sfor<0, 4>([&](auto N) {
        const int id = N * 64 + lane;
        const u32 off = id < NCOMP ? W_SG + id * 512 + 4 : W_CS + ((id < 252 ? id : 251) - NCOMP) * 1024 + 4;
        ok = ok && __hip_atomic_load((GAS u32*)(p.ws + off), __ATOMIC_RELAXED, __HIP_MEMORY_SCOPE_AGENT) == tag;
      });
      if (all(ok)) break;
      __builtin_amdgcn_s_sleep(16);
    }
    if (lane == 0) __hip_atomic_store((GAS u64*)p.ws, epoch, __ATOMIC_RELAXED, __HIP_MEMORY_SCOPE_AGENT);
  }
}

__global__ __launch_bounds__(512, 2) void s5_glu_kernel(const Params p) {
  __shared__ __attribute__((aligned(16))) char lds[163840];
  LAS char* L = (LAS char*)lds;
  u64 code;
  asm volatile("s_getpc_b64 %0" : "=s"(code));
  const int b = blockIdx.x, w = __builtin_amdgcn_readfirstlane(threadIdx.x >> 6), lane = threadIdx.x & 63;
  if (__builtin_expect(b < NCOMP, 1)) {
    const int xcd = b & 7, q = b >> 3;
    const int ti = 4 * (xcd >> 1) + (q & 3), tj = 7 * (xcd & 1) + (q >> 2), rt = 4 * xcd + (q & 3), ct = q >> 2;
    if (__builtin_expect(w < 4, 1)) compute_waves(p, L, w, lane, ti, tj, rt, ct);
    else helper_waves(p, L, w - 4, lane, ti, tj, rt, ct);
  } else {
    support_group(p, L, b - NCOMP, code);
  }
}

}

extern "C" void kernel_launch(void* const* d_in, const int* in_sizes, int n_in, void* d_out, int out_size, void* d_ws,
                              size_t ws_size, hipStream_t stream) {
  Params p;
  p.x = (const u16*)d_in[0];
  p.lre = (const u16*)d_in[1];
  p.lim = (const u16*)d_in[2];
  p.bre = (const u16*)d_in[3];
  p.bim = (const u16*)d_in[4];
  p.cre = (const u16*)d_in[5];
  p.cim = (const u16*)d_in[6];
  p.ldt = (const u16*)d_in[7];
  p.dsk = (const u16*)d_in[8];
  p.wg = (const u16*)d_in[10];
  p.wu = (const u16*)d_in[11];
  p.wd = (const u16*)d_in[12];
  p.out = (float*)d_out;
  p.ws = (char*)d_ws;
  s5_glu_kernel<<<256, 512, 0, stream>>>(p);
}
```

```cpp
#include <hip/hip_runtime.h>
#include <type_traits>

namespace {

typedef unsigned short u16;
typedef unsigned u32;
typedef unsigned long long u64;
typedef short bf16x8 __attribute__((ext_vector_type(8)));
typedef short s16x4 __attribute__((ext_vector_type(4)));
typedef __bf16 bf16x2 __attribute__((ext_vector_type(2)));
typedef float f32x16 __attribute__((ext_vector_type(16)));
typedef float f32x4 __attribute__((ext_vector_type(4)));
typedef float f32x2 __attribute__((ext_vector_type(2)));
typedef u32 v4u __attribute__((ext_vector_type(4)));
typedef u32 v2u __attribute__((ext_vector_type(2)));
typedef __amdgpu_buffer_rsrc_t rsrc_t;
#define LAS __attribute__((address_space(3)))
#define GAS __attribute__((address_space(1)))
#define CAS __attribute__((address_space(4)))
#define DI __device__ __forceinline__

constexpr int SEQ = 2048, DM = 448, FF = 1792;
constexpr int NCOMP = 224;

constexpr u32 slot1(int c) { return (u32)(c & 3) * 28672u; }
constexpr u32 L_AUX = 114688, L_MISC = 147456;
constexpr u32 M_STAT = L_MISC, M_CS = L_MISC + 1024, M_ST2 = L_MISC + 3072, M_DUMMY = L_MISC + 3584,
              M_ZERO = L_MISC + 4096, M_SUB = L_MISC + 6144, M_SUMX = L_MISC + 8192, M_SUMQ = L_MISC + 8704,
              M_CSP = L_MISC + 9216, M_EPOCH = L_MISC + 3968, M_X = L_MISC + 8192;
constexpr u32 M_TR = M_X;
constexpr u32 slotA(int k) { return (u32)((k & 3) == 0 ? 6 : ((k & 3) - 1) * 2) * 16384u; }
constexpr u32 slotB(int k) { return (u32)((k % 5) < 2 ? 7 + (k % 5) : ((k % 5) - 2) * 2 + 1) * 16384u; }

constexpr u32 W_FLAG = 4096, W_SG = 348160, W_ST = 462848, W_CS = 495616, W_H = 557056;
constexpr u32 FLAG_STAGE = 64 * 14 * 128;
constexpr u32 W_END = W_H + 32u * 21u * 16384u;
constexpr u32 W_DIAG = 16u << 20;
constexpr u32 OOB = 0x40000000u;
constexpr u32 SPIN_CAP = 1u << 16;
constexpr int CODE_LINES = 300;

constexpr int DRAIN_A = 156;
constexpr int DRAIN_B = 20;
#define HPRIO 0

struct Params {
  const u16 *x, *lre, *lim, *bre, *bim, *cre, *cim, *ldt, *dsk, *wg, *wu, *wd;
  float* out;
  char* ws;
};

#define IC(v) std::integral_constant<int, (v)>{}
template <int B, int E, class F> DI void sfor(F&& f) {
  if constexpr (B < E) { f(std::integral_constant<int, B>{}); sfor<B + 1, E>(f); }
}
DI float asf(u32 v) { return __builtin_bit_cast(float, v); }
DI u32 asu(float v) { return __builtin_bit_cast(u32, v); }
DI float bf2f(u32 v) { return asf(v << 16); }
DI u32 pk2(float a, float b) { bf16x2 c; c[0] = (__bf16)a; c[1] = (__bf16)b; return __builtin_bit_cast(u32, c); }
DI float rbf(float a) { return bf2f(pk2(a, a) & 0xffffu); }
DI rsrc_t mk(const void* p, u32 bytes) { return __builtin_amdgcn_make_buffer_rsrc((void*)p, (short)0, (int)bytes, 0x00020000); }
DI f32x16 mfma32(bf16x8 a, bf16x8 b, f32x16 c) { return __builtin_amdgcn_mfma_f32_32x32x16_bf16(a, b, c, 0, 0, 0); }
DI f32x4 mfma16(bf16x8 a, bf16x8 b, f32x4 c) { return __builtin_amdgcn_mfma_f32_16x16x32_bf16(a, b, c, 0, 0, 0); }
DI void pin() { __builtin_amdgcn_sched_barrier(0); }
DI void bar() { asm volatile("s_barrier" ::: "memory"); }
template <int N> DI void wait_vm() { asm volatile("s_waitcnt vmcnt(%0)" ::"n"(N) : "memory"); }
DI void wait_lgkm() { asm volatile("s_waitcnt lgkmcnt(0)" ::: "memory"); }

template <class T> DI T ldsr(LAS char* L, u32 off) { return *(LAS T*)(L + off); }
template <class T> DI void ldsw(LAS char* L, u32 off, T v) { *(LAS T*)(L + off) = v; }
DI bf16x8 ldtr(LAS char* L, u32 off, u32 step) {
  const s16x4 lo = __builtin_amdgcn_ds_read_tr16_b64_v4i16((LAS s16x4*)(L + off));
  const s16x4 hi = __builtin_amdgcn_ds_read_tr16_b64_v4i16((LAS s16x4*)(L + off + step));
  return __builtin_shufflevector(lo, hi, 0, 1, 2, 3, 4, 5, 6, 7);
}
template <u32 LO, u32 SO> DI void dma(u32 lbase, u32 voff, rsrc_t rs, u32 sbase) {
  u32 t;
  asm volatile("s_add_u32 m0, %1, %2\n\ts_add_u32 %0, %3, %4\n\tbuffer_load_dwordx4 %5, %6, %0 offen lds"
               : "=&s"(t) : "s"(lbase), "n"(LO), "s"(sbase), "n"(SO), "v"(voff), "s"(rs) : "memory", "scc");
}
template <u32 LO, u32 SO> DI u32 dma_set(u32 lbase, u32 sbase) {
  u32 t;
  asm volatile("s_add_u32 m0, %1, %2\n\ts_add_u32 %0, %3, %4" : "=&s"(t) : "s"(lbase), "n"(LO), "s"(sbase), "n"(SO) : "memory", "scc");
  return t;
}
template <u32 O> DI void dma_go(u32 voff, rsrc_t rs, u32 soff) {
  asm volatile("buffer_load_dwordx4 %0, %1, %2 offen offset:%3 lds" ::"v"(voff), "s"(rs), "s"(soff), "n"(O) : "memory");
}
template <u32 LO, u32 SO> DI void dma4_sc1(u32 lbase, u32 voff, rsrc_t rs, u32 sbase) {
  u32 t;
  asm volatile("s_add_u32 m0, %1, %2\n\ts_add_u32 %0, %3, %4\n\t"
               "buffer_load_dwordx4 %5, %6, %0 offen sc1 lds\n\tbuffer_load_dwordx4 %5, %6, %0 offen offset:1024 sc1 lds\n\t"
               "buffer_load_dwordx4 %5, %6, %0 offen offset:2048 sc1 lds\n\tbuffer_load_dwordx4 %5, %6, %0 offen offset:3072 sc1 lds"
               : "=&s"(t) : "s"(lbase), "n"(LO), "s"(sbase), "n"(SO), "v"(voff), "s"(rs) : "memory", "scc");
}
DI void ald16(u32& d, u32 voff, rsrc_t rs) { asm volatile("buffer_load_ushort %0, %1, %2, 0 offen" : "=v"(d) : "v"(voff), "s"(rs) : "memory"); }
template <u32 O = 0> DI void ald64(v2u& d, u32 voff, rsrc_t rs) { asm volatile("buffer_load_dwordx2 %0, %1, %2, 0 offen offset:%3 sc1" : "=v"(d) : "v"(voff), "s"(rs), "n"(O) : "memory"); }
template <u32 O = 0> DI void ald128(v4u& d, u32 voff, rsrc_t rs) { asm volatile("buffer_load_dwordx4 %0, %1, %2, 0 offen offset:%3 sc1" : "=v"(d) : "v"(voff), "s"(rs), "n"(O) : "memory"); }
DI void ast64(v2u d, u32 voff, rsrc_t rs) { asm volatile("buffer_store_dwordx2 %0, %1, %2, 0 offen sc1" ::"v"(d), "v"(voff), "s"(rs) : "memory"); }
template <u32 O = 0> DI void ast128(v4u d, u32 voff, rsrc_t rs) { asm volatile("buffer_store_dwordx4 %0, %1, %2, 0 offen offset:%3 sc1\n\ts_nop 1" ::"v"(d), "v"(voff), "s"(rs), "n"(O) : "memory"); }
DI float addf(float a, float b) { float d; asm("v_add_f32 %0, %1, %2" : "=v"(d) : "v"(a), "v"(b)); return d; }
DI u32 opaque_s(u32 v) { asm volatile("" : "+s"(v)); return v; }
template <class T> DI void landed(T& v) { asm volatile("" : "+v"(v)); }
DI u32 opaque(u32 v) { asm volatile("" : "+v"(v)); return v; }
DI float pick(float a, float b, u64 m) { float d; asm("v_cndmask_b32 %0, %1, %2, %3" : "=v"(d) : "v"(a), "v"(b), "s"(m)); return d; }

DI bool all(bool c) { return __builtin_amdgcn_ballot_w64(!c) == 0; }

struct cf { float r, i; };
DI void landed(cf& v) { asm volatile("" : "+v"(v.r), "+v"(v.i)); }
DI cf cmul(cf a, cf b) { return {__builtin_fmaf(a.r, b.r, -(a.i * b.i)), __builtin_fmaf(a.r, b.i, a.i * b.r)}; }
DI f32x2 as2(cf v) { landed(v); return f32x2{v.r, v.i}; }
DI cf cfma(cf a, cf b, cf c) { return {__builtin_fmaf(a.r, b.r, __builtin_fmaf(-a.i, b.i, c.r)), __builtin_fmaf(a.r, b.i, __builtin_fmaf(a.i, b.r, c.i))}; }

#define CSTAMP(I)
#define HSTAMP(I)

DI void compute_waves(const Params& p, LAS char* L, const int w, const int lane, const int ti, const int tj, const int rt, const int ct) {
  const u32 lb = (u32)(size_t)L;
  const rsrc_t rs_x = mk(p.x, SEQ * DM * 2), rs_bre = mk(p.bre, DM * DM * 2), rs_bim = mk(p.bim, DM * DM * 2);
  const u32 vx = (8 * w + (lane >> 3)) * 896 + (((lane & 7) ^ ((4 * w + (lane >> 4)) & 7)) << 4);
  const u32 l1 = opaque_s(lb + w * 1024), sx = opaque_s(ti * 128 * 896), sb = opaque_s(tj * 32 * 896);
  auto load_ssm = [&](auto C) {
    constexpr int c = C;
    sfor<0, 4>([&](auto N) { dma<slot1(c) + 4096 * N, 32 * N * 896 + c * 128>(l1, vx, rs_x, sx); });
    dma<slot1(c) + 16384, c * 128>(l1, vx, rs_bre, sb);
    dma<slot1(c) + 20480, c * 128>(l1, vx, rs_bim, sb);
  };
  asm volatile("s_nop 4" ::: "memory");
  load_ssm(IC(0));
  load_ssm(IC(1));
  constexpr int NTOUCH = 0;
  pin();

  const int r = lane & 31, h = lane >> 5;
  const int tau = 16 * ((r >> 2) & 1) + (r & 3) + 4 * (r >> 3);
  const rsrc_t rs_ws = mk(p.ws, W_END), rs_w1 = mk(w >= 2 ? p.wu : p.wg, DM * FF * 2);
  const u32 vw = (lane >> 3) * 3584 + (((lane & 7) ^ ((((lane >> 4) & 1) | (w & 2)) << 1)) << 4);
  const u32 l7 = opaque_s(lb + w * 7168), sw = opaque_s(56 * (w & 1) * 3584 + 128 * tj);
  u32 ax[4], aw[4];
  {
    const int xrow = 32 * w + tau, g = lane >> 4, q = (lane >> 2) & 3, pp = lane & 3;
    const int gsw = ((q >> 1) & 1) | ((pp >> 1) << 1);
    sfor<0, 4>([&](auto S) {
      constexpr int s = S;
      ax[s] = xrow * 128 + (((2 * s + h) ^ ((xrow >> 1) & 7)) << 4);
      aw[s] = opaque((pp >> 1) * 14336 + (8 * (g >> 1) + q) * 128 + (2 * (s ^ gsw) + (pp & 1)) * 16 + 8 * (g & 1));
    });
  }
  const f32x16 zero16 = {0, 0, 0, 0, 0, 0, 0, 0, 0, 0, 0, 0, 0, 0, 0, 0};
  bf16x8 xf[28], wf[6];
  f32x16 acc[2][4];
  const u32 hbase = opaque_s(W_H + ((2 * ti + (w >> 1)) * 21 + (tj >> 1)) * 16384 + ((w & 1) * 8 + 4 * (tj & 1)) * 1024);
  const u32 a_m = opaque(M_STAT + w * 256 + tau * 4);
  const u32 vfs = opaque(lane == 0 ? W_FLAG + ((4 * ti + w) * 14 + tj) * 128 : OOB);
  v2u flagv;

  u32 wbase[2];
  bf16x8 mfrag;
  const u32 a_cs = opaque(M_CS + r * 16);
  auto ffn_read = [&](auto N) {
    constexpr int n = N, y = n % 116, cc = y < 84 ? y / 28 : 3, c = 7 + 4 * (n / 116) + cc;
    constexpr int T = y < 84 ? (y % 28) / 7 : (y - 84) / 8, kk = y < 84 ? y % 7 : (y - 84) % 8;
    if constexpr (kk == 7) {
      wf[n % 6] = ldsr<bf16x8>(L, a_cs + T * 512);
    } else if constexpr (c & 2) {
      if constexpr (kk <= 1 && (n & 1)) wbase[T & 1] = opaque(aw[T] + slot1(c));
      wf[n % 6] = ldtr(L, wbase[T & 1] + 2048 * kk, 512);
    } else {
      wf[n % 6] = ldtr(L, aw[T] + slot1(c) + 2048 * kk, 512);
    }
  };
  wait_vm<6 + NTOUCH>();
  bar();
  sfor<0, 7>([&](auto C) {
    constexpr int c = C;
    CSTAMP(c);
    sfor<0, 4>([&](auto S) { xf[4 * c + S] = ldsr<bf16x8>(L, ax[S] + slot1(c)); });
    if constexpr (c + 2 < 7) load_ssm(IC(c + 2));
    else sfor<0, 7>([&](auto N) { dma<slot1(c + 2) + 1024 * N, (112 * (c - 5) + 8 * N) * 3584>(l7, vw, rs_w1, sw); });
    wait_vm<(c + 2 < 7 ? 6 : 7) + (c == 0 ? NTOUCH : 0)>();
    wait_lgkm();
    bar();
  });
  ffn_read(IC(1)); ffn_read(IC(0)); ffn_read(IC(3)); ffn_read(IC(2));

  float hv[8];
  v4u hp;
  auto epi_op = [&](auto U, auto O) {
    constexpr int u = U, o = O;
    if constexpr (o >= 0 && o < 56) {
      constexpr int T = o / 14, y = o % 14;
      constexpr int e = y < 3 ? y : y == 4 || y == 5 ? y - 1 : y == 7 || y == 8 ? y - 2 : 7;
      if constexpr (y == 3 || y == 6 || y == 9) hp[y / 3 - 1] = opaque(pk2(hv[2 * (y / 3 - 1)], hv[2 * (y / 3 - 1) + 1]));
      else if constexpr (y == 12) hp[3] = opaque(pk2(hv[6], hv[7]));
      else if constexpr (y == 13) __builtin_amdgcn_raw_buffer_store_b128(hp, rs_ws, lane * 16, hbase + (u * 7 * 16 + T) * 1024, 16);
      else if constexpr (y != 11) { hv[e] = acc[u][T][8 * (e >> 2) + (e & 3)] * acc[u][T][8 * (e >> 2) + 4 + (e & 3)]; landed(hv[e]); }
    }
  };

  const int rb = w >> 1, cb = w & 1;
  u32 a2, bw, bc[4];
  bf16x8 af[6], bf[6];
  u32 pa, pb, pc4[4];
  auto p2_addr = [&]() {
    const int ln = opaque(lane);
    const int g = ln >> 4, q = (ln >> 2) & 3, pp = ln & 3, row = 32 * cb + (ln & 31), h = ln >> 5;
    a2 = rb * 8192 + ln * 16;
    bw = (8 * (g >> 1) + q) * 128 + (((4 * cb + 2 * (g & 1) + (pp >> 1)) ^ (((q >> 1) & 1) << 2)) << 4) + 8 * (pp & 1);
    sfor<0, 4>([&](auto S) { constexpr int s = S; bc[s] = row * 128 + (((2 * s + h) ^ ((row >> 1) & 7)) << 4); });
  };
  auto p2_read = [&](auto M) {
    constexpr int m = M, k = m >> 3, ks = m & 7;
    if constexpr (ks == 0) pa = opaque(a2 + slotA(k));
    if constexpr (k < 14) {
      if constexpr (ks == 0) pb = opaque(bw + slotB(k));
      bf[m % 6] = ldtr(L, pb + 2048 * ks, 512);
    } else {
      if constexpr (ks < 4) pc4[ks] = opaque(bc[ks] + slotB(k));
      bf[m % 6] = ldsr<bf16x8>(L, pc4[ks & 3] + (ks >> 2) * 8192);
    }
    af[m % 6] = ldsr<bf16x8>(L, pa + ks * 1024);
  };

  constexpr int E0 = 94;
  float mval;
  sfor<0, 232>([&](auto N) {
    constexpr int n = N, u = n / 116, y = n % 116, cc = y < 84 ? y / 28 : 3;
    constexpr int T = y < 84 ? (y % 28) / 7 : (y - 84) / 8, kk = y < 84 ? y % 7 : (y - 84) % 8;
    if constexpr (n + 4 < 232) {
      constexpr int z = (n + 4) % 116;
      if constexpr (z == 0 || z == 28 || z == 56 || z == 84) {
        constexpr int bi = 7 + 4 * ((n + 4) / 116) + z / 28;
        if constexpr (bi == 8) wait_vm<0>();
        bar();
      }
      ffn_read(IC((n & 1) ? n + 3 : n + 5));
    } else {
      if constexpr (n == 228) { bar(); p2_read(IC(0)); }
      p2_read(IC(n - 227));
    }
    if constexpr (kk == 0) CSTAMP(7 + 16 * u + 4 * cc + T);
    pin();
    if constexpr (kk == 7) acc[u][T] = mfma32(wf[n % 6], mfrag, acc[u][T]);
    else acc[u][T] = mfma32(wf[n % 6], xf[7 * cc + kk], (cc | kk) ? acc[u][T] : zero16);
    pin();
    if constexpr (n == 60) mval = ldsr<float>(L, a_m);
    if constexpr (n == DRAIN_A - 6) flagv = ldsr<v2u>(L, M_EPOCH);
    if constexpr (n == DRAIN_A) { wait_vm<0>(); ast64(flagv, vfs, rs_ws); }
    if constexpr (n == 200) p2_addr();
    if constexpr (n == 66) {
      const u32 mb = h ? 0u : (asu(mval) >> 16) ^ 0x8000u;
      mfrag = __builtin_bit_cast(bf16x8, v4u{mb * 0x10001u, mb, 0u, 0u});
      landed(mfrag);
    }
    if constexpr (n >= E0) sfor<0, 2>([&](auto J) { epi_op(IC(0), IC(7 * (n - E0) / 4 + J < 7 * (n + 1 - E0) / 4 ? 7 * (n - E0) / 4 + J : -1)); });
    if constexpr (n >= 116 + E0) sfor<0, 2>([&](auto J) { epi_op(IC(1), IC(7 * (n - 116 - E0) / 4 + J < 7 * (n + 1 - 116 - E0) / 4 ? 7 * (n - 116 - E0) / 4 + J : -1)); });
    pin();
  });
  CSTAMP(39);

  f32x16 accF, accY;
  u32 dv;
  float xv[16], r2[16], sk[16];
  f32x4 st;
  const int l2 = opaque(lane);
  const int trow = 64 * rt + 32 * rb + 16 * (l2 >> 5), dcol = 64 * ct + 32 * cb + (l2 & 31);
  const u32 sbase = opaque(M_ST2 + (32 * rb + 16 * (l2 >> 5)) * 8);
  const u32 xbase = opaque(M_X + (32 * rb + 16 * (l2 >> 5)) * 128 + (32 * cb + (l2 & 31)) * 2);
  const rsrc_t rs_out = mk(p.out, SEQ * DM * 4);
  const u32 eoff = trow * DM + dcol;
  sfor<0, 168>([&](auto M) {
    constexpr int m = M, k = m >> 3;
    if constexpr (m + 5 < 168) {
      if constexpr ((m + 5) % 8 == 0) bar();
      p2_read(IC(m + 5));
    }
    if constexpr ((m & 7) == 0) CSTAMP(40 + k);
    pin();
    if constexpr (k < 14) accF = mfma32(af[m % 6], bf[m % 6], m ? accF : zero16);
    else accY = mfma32(af[m % 6], bf[m % 6], m > 112 ? accY : zero16);
    pin();
    if constexpr (m < 6) sfor<0, 3>([&](auto J) { epi_op(IC(1), IC(7 * (116 - E0) / 4 + 3 * m + J)); });
    if constexpr (m == DRAIN_B) {
      wait_vm<0>();
      ast64(flagv, vfs + 2 * FLAG_STAGE, rs_ws);
      dv = ((const GAS u16*)p.dsk)[dcol];
    }
    if constexpr (m >= 40 && m < 104) {
      constexpr int e = (m - 40) / 4, y = (m - 40) % 4;
      if constexpr (y == 0) {
        if constexpr ((e & 1) == 0) st = ldsr<f32x4>(L, sbase + e * 8);
        xv[e] = bf2f(ldsr<u16>(L, xbase + e * 128));
      }
      if constexpr (y == 2) {
        const float mm = st[2 * (e & 1)], rr = st[2 * (e & 1) + 1];
        sk[e] = rbf(rbf(rbf(xv[e] - mm) * rr) * bf2f(dv));
        r2[e] = rr * rr;
        landed(sk[e]); landed(r2[e]);
      }
    }
    if constexpr (m >= 114 && m < 162 && (m - 114) % 3 == 0) {
      constexpr int e = (m - 114) / 3;
      const float o = rbf(accF[e] * r2[e]);
      r2[e] = rbf(o * __builtin_amdgcn_rcpf(1.f + __expf(-o)));
      xv[e] = __builtin_fmaf(sk[e], r2[e], xv[e]);
      landed(r2[e]); landed(xv[e]);
    }
    pin();
  });

  CSTAMP(61);
  sfor<0, 16>([&](auto E) {
    constexpr int e = E;
    __builtin_amdgcn_raw_buffer_store_b32(asu(__builtin_fmaf(accY[e], r2[e], xv[e])), rs_out, eoff * 4, e * DM * 4, 16);
  });
}

DI void helper_waves(const Params& p, LAS char* L, const int w, const int lane, const int ti, const int tj, const int rt, const int ct) {
  const int pc = lane & 31, h = lane >> 5;
  const u32 lb = (u32)(size_t)L;
  const rsrc_t rs_ws = mk(p.ws, W_END);
  u32 plr, pli, pdt;
  v2u ep;
  ald16(plr, (32 * tj + pc) * 2, mk(p.lre, DM * 2));
  ald16(pli, (32 * tj + pc) * 2, mk(p.lim, DM * 2));
  ald16(pdt, (32 * tj + pc) * 2, mk(p.ldt, DM * 2));
#define SYNC(N, I) do { pin(); wait_vm<N>(); wait_lgkm(); bar(); pin(); } while (0)
#define SBAR(I) bar()
  const rsrc_t rs_w1 = mk(w >= 2 ? p.wu : p.wg, DM * FF * 2), rs_wd = mk(p.wd, FF * DM * 2);
  const rsrc_t rs_cre = mk(p.cre, DM * DM * 2), rs_cim = mk(p.cim, DM * DM * 2);
  const u32 vx = (8 * w + (lane >> 3)) * 896 + (((lane & 7) ^ ((4 * w + (lane >> 4)) & 7)) << 4);
  const u32 vw = (lane >> 3) * 3584 + (((lane & 7) ^ ((((lane >> 4) & 1) | (w & 2)) << 1)) << 4);
  const u32 va = lane * 16;
  const u32 vd = (lane >> 3) * 896 + (((lane & 7) ^ (((lane >> 4) & 1) << 2)) << 4);
  const u32 l1 = opaque_s(lb + w * 1024), l7 = opaque_s(lb + w * 7168), l4 = opaque_s(lb + w * 4096);
  const u32 sw = opaque_s(56 * (w & 1) * 3584 + 128 * tj), sa = opaque_s(W_H + rt * 21 * 16384 + w * 4096);
  const u32 sd = opaque_s(32 * w * 896 + ct * 128), sc = opaque_s(64 * ct * 896);
  u32 vw4[4], vd4[4], so;
  sfor<0, 4>([&](auto N) { vw4[N] = opaque(vw + N * (8 * 3584 - 1024)); vd4[N] = opaque(vd + N * (8 * 896 - 1024)); });
  auto ldw = [&](auto C, auto N) {
    constexpr int c = C, n = N, u = (c - 7) / 4, cc = (c - 7) % 4;
    if constexpr ((n & 3) == 0) so = dma_set<slot1(c) + 1024 * n, (112 * cc + 8 * n) * 3584 + u * 1792>(l7, sw);
    dma_go<1024 * (n & 3)>(vw4[n & 3], rs_w1, so);
    pin();
  };
  auto ldd = [&](auto K, auto N) {
    if constexpr (N == 0) so = dma_set<slotB(K), 128 * K * 896>(l4, sd);
    dma_go<1024 * N>(vd4[N], rs_wd, so);
    pin();
  };
  auto loadA = [&](auto K) { dma4_sc1<slotA(K), K * 16384>(l4, va, rs_ws, sa); };
  auto loadB = [&](auto K) {
    constexpr int k = K;
    if constexpr (k < 14) {
      sfor<0, 4>([&](auto N) { ldd(K, N); });
    } else {
      sfor<0, 2>([&](auto N) {
        dma<slotB(k) + 4096 * N, 32 * N * 896 + (k - 14) * 128>(l1, vx, rs_cre, sc);
        dma<slotB(k) + 8192 + 4096 * N, 32 * N * 896 + (k - 14) * 128>(l1, vx, rs_cim, sc);
      });
    }
  };

  const int tau = 16 * ((pc >> 2) & 1) + (pc & 3) + 4 * (pc >> 3);
  u32 ax[4], ab[4];
  sfor<0, 4>([&](auto S) {
    const int s = (S + w) & 3, xrow = 32 * w + tau;
    ax[S] = opaque(xrow * 128 + (((2 * s + h) ^ ((xrow >> 1) & 7)) << 4));
    ab[S] = opaque(pc * 128 + (((2 * s + h) ^ ((pc >> 1) & 7)) << 4) + 16384);
  });
  const short one = ((lane & 15) == ((lane >> 4) & 1)) ? (short)0x3f80 : (short)0;
  bf16x8 sel = {one, one, one, one, one, one, one, one};
  landed(sel);
  const u32 a_csp = opaque(M_CSP + pc * 4), a_sub = opaque(M_SUB + pc * 8), a_own = opaque(M_SUB + (2 * w + h) * 256 + pc * 8);
  const u32 a_csw = opaque(lane < 16 ? M_CSP + w * 256 + lane * 4 : M_DUMMY);
  const u32 a_sq = opaque(h == ((pc >> 2) & 1) ? M_SUMQ + w * 128 + tau * 4 : M_DUMMY + lane * 4);
  const u32 a_sx = opaque((lane & 15) < 2 ? M_SUMX + w * 128 + (16 * ((lane >> 4) & 1) + 8 * (lane & 1) + 4 * (lane >> 5)) * 4 : M_DUMMY + 256);
  const u32 a_stw = opaque(M_STAT + w * 256 + h * 128 + pc * 4), a_str = opaque(M_STAT + w * 256 + h * 64);
  const int d0 = ti - 1 - 2 * (2 * w + h);
  const u32 vs0 = opaque(d0 >= 0 ? W_SG + (tj * 16 + d0) * 512 + pc * 16 : OOB);
  const u32 vs1 = opaque(d0 >= 1 ? W_SG + (tj * 16 + d0 - 1) * 512 + pc * 16 : OOB);
  const u32 vsw = opaque(w == 3 && h ? W_SG + (tj * 16 + ti) * 512 + pc * 16 : OOB);
  const u32 vst = opaque(tj == 0 && lane < 32 ? W_ST + (ti * 128 + w * 32 + lane) * 16 : OOB);
  const u32 vcs = opaque(W_CS + tj * 2048 + (32 * w + pc) * 16);
  const u32 a_csf = opaque(M_CS + (2 * (w & 1) + (pc >> 4)) * 512 + (16 * ((pc >> 2) & 1) + 4 * ((pc >> 3) & 1) + 8 * (w >> 1) + (pc & 3)) * 16);
  const u32 vfl = opaque(W_FLAG + (rt * 28 + (lane < 28 ? lane : 27)) * 128);
  const u32 vfs = opaque(lane == 0 ? W_FLAG + FLAG_STAGE + ((4 * ti + w) * 14 + tj) * 128 : OOB);
  const u32 a_tw = opaque(M_TR + w * 2048 + pc * 64 + (((2 * h) ^ ((pc >> 1) & 3)) << 4));
  const u32 a_tr = opaque(M_TR + w * 2048 + (8 * h + ((lane >> 2) & 3)) * 64 + (((2 * (lane & 1) + ((lane >> 4) & 1)) ^ ((lane >> 3) & 1)) << 4) + 8 * ((lane >> 1) & 1));
  const u32 vhs = opaque(W_H + ((2 * ti + (w >> 1)) * 21 + 14 + (tj >> 1)) * 16384 + ((w & 1) * 8 + 2 * (tj & 1)) * 1024 + va);
  ldsw<f32x2>(L, a_own - 2048, f32x2{0.f, 0.f});
  cf lam, lam4, lam16, lam128, lamk, lamp, coef, l32, l64, l256, l512, c0, c1;
  float lr, li, dt, t0, t1, t2, t3, th, kq;
  int iq;
  v2u flagv;
  const cf unit = {1.f, 0.f};
  constexpr float L2E = 1.44269502e+00f, L2E_LO = 1.92596299e-08f, LN2 = 6.93147182e-01f;
  auto sq = [&](cf a) { return cf{__builtin_fmaf(a.r, a.r, -(a.i * a.i)), (a.r + a.r) * a.i}; };
  auto lam_op = [&](auto O) {
    constexpr int o = O;
    if constexpr (o == 0) { wait_vm<0>(); landed(plr); landed(pli); landed(pdt); lr = bf2f(plr); li = bf2f(pli); t0 = bf2f(pdt); t1 = t0 * L2E; landed(t1); }
    if constexpr (o == 1 || o == 4) { t2 = __builtin_fmaf(t0, L2E_LO, __builtin_fmaf(t0, L2E, -t1)) * LN2; t1 = __builtin_amdgcn_exp2f(t1); landed(t1); landed(t2); }
    if constexpr (o == 2) { dt = rbf(__builtin_fmaf(t1, t2, t1)); landed(dt); ald64(ep, opaque(0), rs_ws); }
    if constexpr (o == 3) { t0 = dt * lr; th = dt * li; t1 = t0 * L2E; kq = __builtin_rintf(th * 0.636619772f); landed(t1); landed(kq); }
    if constexpr (o == 5) {
      dt = __builtin_fmaf(t1, t2, t1);
      th = __builtin_fmaf(kq, -5.39030253e-15f, __builtin_fmaf(kq, -3.13916473e-07f, __builtin_fmaf(kq, -1.57079601e+00f, th)));
      landed(dt); landed(th);
    }
    if constexpr (o == 6) { t0 = th * th; t1 = __builtin_fmaf(t0, __builtin_fmaf(t0, __builtin_fmaf(t0, 2.86567956e-6f, -1.98559923e-4f), 8.33338592e-3f), -1.66666672e-1f); landed(t1); }
    if constexpr (o == 7) { t1 = __builtin_fmaf(th * t0, t1, th); t2 = __builtin_fmaf(t0, __builtin_fmaf(t0, 2.44677067e-5f, -1.38877297e-3f), 4.16666567e-2f); landed(t1); landed(t2); }
    if constexpr (o == 8) { t2 = __builtin_fmaf(t0, __builtin_fmaf(t0, t2, -0.5f), 1.f); iq = (int)kq; t3 = __builtin_fmaf(lr, lr, li * li); landed(t2); landed(iq); landed(t3); }
    if constexpr (o == 9) { const bool odd = iq & 1; t0 = odd ? t2 : t1; t1 = odd ? t1 : t2; landed(t0); landed(t1); }
    if constexpr (o == 10) { t0 = asf(asu(t0) ^ ((u32)(iq & 2) << 30)); t3 = __builtin_amdgcn_rcpf(t3); landed(t0); landed(t3); }
    if constexpr (o == 11) { t1 = asf(asu(t1) ^ ((u32)((iq + 1) & 2) << 30)); landed(t1); }
    if constexpr (o == 12) { lam = cf{dt * t1, dt * t0}; t3 += t3; t0 = lam.r - 1.f; landed(lam); landed(t3); landed(t0); }
    if constexpr (o == 13) { coef = cf{__builtin_fmaf(t0, lr, lam.i * li), __builtin_fmaf(lam.i, lr, -(t0 * li))}; landed(coef); }
    if constexpr (o == 14) { coef = cf{coef.r * t3, coef.i * t3}; landed(coef); }
    if constexpr (o == 15) { lam4 = sq(lam); landed(lam4); }
    if constexpr (o == 16) { lam4 = sq(lam4); landed(lam4); }
    if constexpr (o == 17) { lam16 = sq(lam4); landed(lam16); }
    if constexpr (o == 18) { lam16 = sq(lam16); landed(lam16); }
    if constexpr (o == 19) { l32 = sq(lam16); landed(l32); }
    if constexpr (o == 20) { l64 = sq(l32); landed(l64); }
    if constexpr (o == 21) { lam128 = sq(l64); landed(lam128); }
    if constexpr (o == 22) { l256 = sq(lam128); landed(l256); }
    if constexpr (o == 23) { l512 = sq(l256); landed(l512); }
    if constexpr (o == 24) { c0 = h ? lam16 : unit; c1 = (w & 1) ? l32 : unit; landed(c0); landed(c1); }
    if constexpr (o == 25) { lamk = cmul(c0, c1); landed(lamk); }
    if constexpr (o == 26) { c0 = (w & 2) ? l64 : unit; c1 = h ? l256 : unit; landed(c0); landed(c1); }
    if constexpr (o == 27) { lamk = cmul(lamk, c0); landed(lamk); }
    if constexpr (o == 28) { lamk = cmul(lamk, coef); landed(lamk); }
    if constexpr (o == 29) { c0 = (w & 1) ? l512 : unit; landed(c0); }
    if constexpr (o == 30) { lamp = cmul(c0, c1); landed(lamp); }
    if constexpr (o == 31) { c0 = sq(l512); landed(c0); }
    if constexpr (o == 32) { c0 = (w & 2) ? c0 : unit; landed(c0); }
    if constexpr (o == 33) { lamp = cmul(lamp, c0); landed(lamp); }
    if constexpr (o == 34) {
      wait_vm<0>();
      landed(ep);
      const u32 lo = ep[0] + 1;
      flagv = v2u{lo, ep[1] + (lo == 0)};
      ldsw<v2u>(L, M_EPOCH, flagv);
    }
  };
  const f32x16 zero16 = {0, 0, 0, 0, 0, 0, 0, 0, 0, 0, 0, 0, 0, 0, 0, 0};
  const f32x4 zero4 = {0, 0, 0, 0};
  bf16x8 xf[3], bre[3], bim[3];
  f32x16 are, aim, gram;
  f32x4 sum, csr, csi;
  u32 xa, ba;
  auto ssm_read = [&](auto K, auto J) {
    constexpr int ks = K, c = ks >> 2, s = ks & 3;
    if constexpr (ks < 28) {
      if constexpr (J == 0) { ba = (c & 2) ? opaque(ab[s] + slot1(c)) : ab[s] + slot1(c); bim[ks % 3] = ldsr<bf16x8>(L, ba + 4096); }
      if constexpr (J == 1) bre[ks % 3] = ldsr<bf16x8>(L, ba);
      if constexpr (J == 2) { xa = (c & 2) ? opaque(ax[s] + slot1(c)) : ax[s] + slot1(c); xf[ks % 3] = ldsr<bf16x8>(L, xa); }
    }
  };
  wait_lgkm();
  bar();
  sfor<0, 6>([&](auto J) { ssm_read(IC(J / 3), IC(J % 3)); });
  sfor<0, 28>([&](auto K) {
    constexpr int ks = K, o = 3 * (ks - 3);
    pin();
    gram = mfma32(xf[ks % 3], xf[ks % 3], ks ? gram : zero16);
    pin();
    if constexpr (((ks + 2) & 3) == 0) {
      if constexpr (ks == 26) wait_lgkm();
      bar();
    }
    ssm_read(IC(ks + 2), IC(0));
    lam_op(IC(o));
    if constexpr (ks == 25) { ldsw<float>(L, a_csw, csr[0]); ldsw<float>(L, a_csw + 64, csr[1]); }
    pin();
    are = mfma32(xf[ks % 3], bre[ks % 3], ks ? are : zero16);
    pin();
    ssm_read(IC(ks + 2), IC(1));
    lam_op(IC(o + 1));
    if constexpr (ks == 25) { ldsw<float>(L, a_csw + 128, csi[0]); ldsw<float>(L, a_csw + 192, csi[1]); }
    pin();
    aim = mfma32(xf[ks % 3], bim[ks % 3], ks ? aim : zero16);
    pin();
    ssm_read(IC(ks + 2), IC(2));
    lam_op(IC(o + 2));
    pin();
    sum = mfma16(xf[ks % 3], sel, ks ? sum : zero4);
    if constexpr ((ks & 3) == 0) {
      csr = mfma16(sel, bre[ks % 3], ks ? csr : zero4);
      csi = mfma16(sel, bim[ks % 3], ks ? csi : zero4);
    }
    pin();
  });
  const u32 tag = flagv[0];

  v4u cg;
  auto cs_issue = [&](int u) { ald128(cg, vcs + u * 28672, rs_ws); };
  auto cs_finish = [&](int u) {
    landed(cg);
#pragma unroll 1
    for (u32 n = 0; n < SPIN_CAP && !all(cg[1] == tag && cg[3] == tag); ++n) {
      __builtin_amdgcn_s_sleep(2);
      cs_issue(u); wait_vm<0>(); landed(cg);
    }
    ldsw<v4u>(L, a_csf, v4u{cg[0], cg[2], 0u, 0u});
  };
  v2u fl;
  auto flag_issue = [&](int stage) { ald64(fl, vfl + stage * FLAG_STAGE, rs_ws); };
  auto flag_finish = [&](int stage) {
    landed(fl);
#pragma unroll 1
    for (u32 n = 0; n < SPIN_CAP && !all(fl[0] == flagv[0] && fl[1] == flagv[1]); ++n) {
      __builtin_amdgcn_s_sleep(2);
      flag_issue(stage); wait_vm<0>(); landed(fl);
    }
  };

  __builtin_amdgcn_s_setprio(HPRIO);
  cf b[4][4];
  f32x4 mm[4], rr[4];
  f32x2 stat;
  cf cc;
  {
    float t[8], gd[8];
    sfor<0, 8>([&](auto N) { t[N] = ldsr<float>(L, a_csp + N * 128); });
    ldw(IC(9), IC(0));
    cc = cf{(t[0] + t[2]) + (t[4] + t[6]), (t[1] + t[3]) + (t[5] + t[7])};
    landed(cc);
    ldsw<f32x4>(L, a_sx, sum);
    ldw(IC(9), IC(1));
    asm volatile("s_nop 7\n\ts_nop 3" : "+v"(gram));
    sfor<0, 8>([&](auto N) { constexpr int n = N; gd[n] = pick(gram[2 * n], gram[2 * n + 1], 0xaaaaaaaaaaaaaaaaull); });
    sfor<0, 4>([&](auto N) { gd[N] = pick(gd[2 * N], gd[2 * N + 1], 0xccccccccccccccccull); });
    sfor<0, 2>([&](auto N) { gd[N] = pick(gd[2 * N], gd[2 * N + 1], 0xff00ff00ff00ff00ull); });
    ldsw<float>(L, a_sq, pick(gd[0], gd[1], 0xffff0000ffff0000ull));
    const float sx = ldsr<float>(L, a_csp + (M_SUMX - M_CSP) + w * 128), sq = ldsr<float>(L, a_csp + (M_SUMQ - M_CSP) + w * 128);
    ldw(IC(9), IC(2));
    float mean = sx * (1.f / DM);
    landed(mean);
    const float var = fmaxf(__builtin_fmaf(sq, 1.f / DM, -(mean * mean)), 0.f);
    stat = f32x2{rbf(mean), __builtin_amdgcn_rcpf(rbf(__builtin_amdgcn_sqrtf(rbf(rbf(var) + 1e-5f))))};
    ldsw<float>(L, a_stw, h ? stat[1] : stat[0]);
    sfor<0, 4>([&](auto G) { mm[G] = ldsr<f32x4>(L, a_str + G * 16); rr[G] = ldsr<f32x4>(L, a_str + 128 + G * 16); });
    sfor<3, 7>([&](auto N) { ldw(IC(9), N); });
  }
  SYNC(7, 8);

  sfor<0, 4>([&](auto G) {
    constexpr int g = G;
    ldw(IC(10), IC(2 * g));
    sfor<0, 4>([&](auto E) {
      constexpr int e = E;
      b[g][e] = cf{__builtin_fmaf(-mm[g][e], cc.r, are[4 * g + e]), __builtin_fmaf(-mm[g][e], cc.i, aim[4 * g + e])};
    });
    if constexpr (g < 3) ldw(IC(10), IC(2 * g + 1));
    sfor<0, 4>([&](auto E) { constexpr int e = E; b[g][e] = cf{b[g][e].r * rr[g][e], b[g][e].i * rr[g][e]}; landed(b[g][e]); });
  });
  SYNC(7, 9);

  cs_issue(0);
  cf en[4], fin;
  sfor<0, 4>([&](auto G) { en[G] = b[G][0]; });
  sfor<1, 4>([&](auto E) {
    constexpr int e = E;
    ldw(IC(11), IC(2 * e - 2));
    sfor<0, 2>([&](auto G) { en[G] = cfma(lam, en[G], b[G][e]); });
    ldw(IC(11), IC(2 * e - 1));
    sfor<2, 4>([&](auto G) { en[G] = cfma(lam, en[G], b[G][e]); });
  });
  ldw(IC(11), IC(6));
  fin = cfma(lam4, cfma(lam4, cfma(lam4, en[0], en[1]), en[2]), en[3]);
  ldsw<f32x2>(L, a_own, as2(fin));
  wait_vm<7>();
  cs_finish(0);
  SYNC(7, 10);

  cf pre;
  {
    f32x2 v[7];
    sfor<0, 7>([&](auto K) { v[K] = ldsr<f32x2>(L, a_own - (7 - K) * 256); });
    ldw(IC(12), IC(0));
    pre = cf{v[0][0], v[0][1]};
    sfor<1, 7>([&](auto K) {
      pre = cfma(lam16, pre, cf{v[K][0], v[K][1]});
      if constexpr (K & 1) ldw(IC(12), IC(1 + K / 2));
    });
    fin = cfma(lam16, pre, fin);
    landed(fin);
    ast128(v4u{asu(fin.r), tag, asu(fin.i), tag}, vsw, rs_ws);
    ast128(v4u{asu(stat[0]), tag, asu(stat[1]), tag}, vst, rs_ws);
    sfor<4, 7>([&](auto N) { ldw(IC(12), N); });
    pre = cmul(coef, pre);
    sfor<0, 3>([&](auto G) { en[G] = cmul(coef, en[G]); landed(en[G]); });
    landed(pre);
  }
  SYNC(9, 11);

  sfor<0, 4>([&](auto G) {
    ldw(IC(13), IC(2 * G));
    sfor<0, 2>([&](auto E) { b[G][E] = cmul(coef, b[G][E]); });
    if constexpr (G < 3) ldw(IC(13), IC(2 * G + 1));
    sfor<2, 4>([&](auto E) { b[G][E] = cmul(coef, b[G][E]); });
    sfor<0, 4>([&](auto E) { landed(b[G][E]); });
  });
  SYNC(7, 12);

  v4u s0, s1;
  ald128(s0, vs0, rs_ws); ald128(s1, vs1, rs_ws);
  cs_issue(1);
  sfor<0, 7>([&](auto N) { ldw(IC(14), N); });
  wait_vm<7>();
  landed(s0); landed(s1);
  HSTAMP(80);
#pragma unroll 1
  for (u32 n = 0; n < SPIN_CAP && !all((d0 < 0 || (s0[1] == tag && s0[3] == tag)) && (d0 < 1 || (s1[1] == tag && s1[3] == tag))); ++n) {
    __builtin_amdgcn_s_sleep(2);
    ald128(s0, vs0, rs_ws); ald128(s1, vs1, rs_ws); wait_vm<0>(); landed(s0); landed(s1);
  }
  HSTAMP(81);
  {
    const cf t = cmul(lamp, cfma(lam128, cf{asf(s1[0]), asf(s1[2])}, cf{asf(s0[0]), asf(s0[2])}));
    ldsw<f32x2>(L, a_own, as2(t));
  }
  cs_finish(1);
  SYNC(7, 13);

  HSTAMP(82);
  flag_issue(0);
  u32 pr[8], pi[8];
  cf s2[4];
  {
    f32x2 v[8];
    sfor<0, 8>([&](auto K) { v[K] = ldsr<f32x2>(L, a_sub + K * 256); });
    cf t4[4], t;
    sfor<0, 4>([&](auto K) { t4[K] = cf{addf(v[2 * K][0], v[2 * K + 1][0]), addf(v[2 * K][1], v[2 * K + 1][1])}; });
    sfor<0, 2>([&](auto K) { t4[K] = cf{addf(t4[2 * K].r, t4[2 * K + 1].r), addf(t4[2 * K].i, t4[2 * K + 1].i)}; });
    t = cf{addf(t4[0].r, t4[1].r), addf(t4[0].i, t4[1].i)};
    s2[0] = cfma(lamk, t, pre);
    sfor<1, 4>([&](auto G) { s2[G] = cfma(lam4, s2[G - 1], en[G - 1]); });
    landed(s2[3]);
  }
  HSTAMP(83);
  wait_vm<0>();
  HSTAMP(84);
  flag_finish(0);
  HSTAMP(85);
  loadA(IC(0));
  loadB(IC(0));
  HSTAMP(86);
  SYNC(8, 14);
  HSTAMP(87);

  auto pass2 = [&](auto J) {
    constexpr int j = J;
    cf o[4];
    sfor<0, 4>([&](auto G) { o[G] = cfma(lam, s2[G], b[G][2 * j]); });
    sfor<0, 4>([&](auto G) {
      s2[G] = cfma(lam, o[G], b[G][2 * j + 1]);
      landed(o[G]); landed(s2[G]);
      pr[2 * G + j] = opaque(pk2(o[G].r, s2[G].r));
      pi[2 * G + j] = opaque(pk2(-o[G].i, -s2[G].i));
    });
  };
  auto hs_out = [&](const u32* v, u32 voff) {
    ldsw<v4u>(L, a_tw, v4u{v[0], v[1], v[2], v[3]});
    ldsw<v4u>(L, a_tw ^ 16, v4u{v[4], v[5], v[6], v[7]});
    bf16x8 f[2];
    sfor<0, 2>([&](auto Z) {
      const s16x4 lo = __builtin_amdgcn_ds_read_tr16_b64_v4i16((LAS s16x4*)(L + a_tr + Z * 1024));
      const s16x4 hi = __builtin_amdgcn_ds_read_tr16_b64_v4i16((LAS s16x4*)(L + (a_tr ^ 32) + 256 + Z * 1024));
      f[Z] = __builtin_shufflevector(lo, hi, 0, 1, 2, 3, 4, 5, 6, 7);
    });
    ast128<0>(__builtin_bit_cast(v4u, f[0]), voff, rs_ws);
    ast128<1024>(__builtin_bit_cast(v4u, f[1]), voff, rs_ws);
  };
  loadA(IC(1));
  loadB(IC(1));
  pass2(IC(0));
  SYNC(8, 15);
  __builtin_amdgcn_s_setprio(0);

  loadB(IC(2)); loadA(IC(2));
  pass2(IC(1));
  SYNC(8, 16);
  loadA(IC(3)); loadB(IC(3)); loadB(IC(4));
  hs_out(pr, vhs);
  SYNC(14, 17);
  loadA(IC(4)); loadB(IC(5));
  hs_out(pi, vhs + 4096);
  SYNC(16, 18);
  v4u st2;
  const u32 vst2 = w == 2 ? W_ST + (rt * 64 + lane) * 16 : OOB;
  loadA(IC(5)); loadB(IC(6));
  {
    const u32 vxt = (8 * w + (lane >> 3)) * 896 + (lane & 7) * 16, sxt = rt * 64 * 896 + ct * 128;
    dma<M_X, 0>(l1, vxt, mk(p.x, SEQ * DM * 2), sxt);
    dma<M_X + 4096, 32 * 896>(l1, vxt, mk(p.x, SEQ * DM * 2), sxt);
  }
  ald128(st2, vst2, rs_ws);
  SYNC(17, 19);
  sfor<4, 20>([&](auto KK) {
    constexpr int k = KK;
    if constexpr (k == 4 || k == 11) { if (w == 0) flag_issue(k == 4 ? 2 : 1); }
    if constexpr (k + 2 < 21) loadA(IC(k + 2));
    if constexpr (k + 3 < 21) loadB(IC(k + 3));
    if constexpr (k == 5) ast64(flagv, vfs, rs_ws);
    constexpr int cnt = (k + 2 < 21 ? 4 : 0) + (k + 3 < 21 ? 4 : 0) + (k == 5 ? 1 : k == 6 ? 5 : 0);
    if constexpr (k == 4 || k == 11) { wait_vm<cnt>(); if (w == 0) flag_finish(k == 4 ? 2 : 1); }
    if constexpr (k == 4) {
      landed(st2);
      if (w == 2) {
#pragma unroll 1
        for (u32 n = 0; n < SPIN_CAP && !all(st2[1] == tag && st2[3] == tag); ++n) {
          __builtin_amdgcn_s_sleep(2);
          ald128(st2, vst2, rs_ws); wait_vm<0>(); landed(st2);
        }
        ldsw<v2u>(L, M_ST2 + lane * 8, v2u{st2[0], st2[2]});
      }
    }
    SYNC(cnt, k + 16);
  });
}

DI void support_group(const Params& p, LAS char* L, const int s, const u64 code) {
  const int tid = threadIdx.x, lane = tid & 63, w = tid >> 6;
  u32 sink = 0;
  auto touch = [&](const GAS char* a) { asm volatile("global_load_dword %0, %1, off" : "+v"(sink) : "v"(a) : "memory"); };
  if (tid < CODE_LINES / 4) touch((const GAS char*)code + ((s >> 3) + 4 * tid) * 128);
  const u64 epoch = *(const CAS u64*)p.ws + 1;
  const u32 tag = (u32)epoch;
  if (s < 28) {
    const int j = 7 * (s & 1) + (s >> 1) % 7, mat = (s >> 1) / 7;
    const GAS char* W = (const GAS char*)(mat ? p.wu : p.wg) + (56 * w + (lane >> 3)) * 3584 + j * 128 + (lane & 7) * 16;
    v4u v[2][7];
    sfor<0, 14>([&](auto N) { v[N / 7][N % 7] = *(const GAS v4u*)(W + (N / 7) * 1792 + (N % 7) * 8 * 3584); });
    pin();
    sfor<0, 2>([&](auto U) {
      constexpr int u = U;
      float a[8];
      sfor<0, 4>([&](auto E) {
        constexpr int e = E;
        float lo = 0.f, hi = 0.f;
        sfor<0, 7>([&](auto N) { lo += bf2f(v[u][N][e]); hi += asf(v[u][N][e] & 0xffff0000u); });
        a[2 * e] = lo; a[2 * e + 1] = hi;
      });
      sfor<0, 8>([&](auto E) {
        a[E] += __shfl_xor(a[E], 8); a[E] += __shfl_xor(a[E], 16); a[E] += __shfl_xor(a[E], 32);
      });
      if (lane < 8) {
        ldsw<f32x4>(L, u * 2048 + w * 256 + lane * 32, f32x4{a[0], a[1], a[2], a[3]});
        ldsw<f32x4>(L, u * 2048 + w * 256 + lane * 32 + 16, f32x4{a[4], a[5], a[6], a[7]});
      }
      wait_lgkm();
      bar();
      if (w == 0) {
        float t = 0.f;
        sfor<0, 8>([&](auto N) { t += ldsr<float>(L, u * 2048 + N * 256 + lane * 4); });
        const float t0 = rbf(t), t1 = rbf(t - t0);
        __builtin_amdgcn_raw_buffer_store_b128(v4u{pk2(t0, t1), tag, pk2(t - t0 - t1, 0.f), tag}, mk(p.ws, W_END),
                                               W_CS + (u * 14 + j) * 2048 + mat * 1024 + lane * 16, 0, 16);
      }
    });
  }
  sfor<0, 2>([&](auto N) {
    const int o = N * 512 + tid;
    if (o < 588) {
      const int ln = s * 588 + o;
      touch(ln < 12544 ? (const GAS char*)p.wd + ln * 128 : ln < 15680 ? (const GAS char*)p.cre + (ln - 12544) * 128 : (const GAS char*)p.cim + (ln - 15680) * 128);
    }
  });
  if (lane < 28) {
    const int ln = (s >> 3) * 224 + w * 28 + lane;
    sfor<0, 21>([&](auto K) {
      constexpr int k = K;
      if constexpr (k < 14) touch((const GAS char*)p.wd + (k * 896 + ln) * 128);
      else touch((const GAS char*)(ln < 448 ? p.cre : p.cim) + (ln < 448 ? ln : ln - 448) * 896 + (k - 14) * 128);
    });
  }
  asm volatile("s_waitcnt vmcnt(0)" : "+v"(sink)::"memory");
  if (s == 31 && w == 0) {
#pragma unroll 1
    for (u32 n = 0; n < SPIN_CAP; ++n) {
      bool ok = true;
      sfor<0, 4>([&](auto N) {
        const int id = N * 64 + lane;
        const u32 off = id < NCOMP ? W_SG + id * 512 + 4 : W_CS + ((id < 252 ? id : 251) - NCOMP) * 1024 + 4;
        ok = ok && __hip_atomic_load((GAS u32*)(p.ws + off), __ATOMIC_RELAXED, __HIP_MEMORY_SCOPE_AGENT) == tag;
      });
      if (all(ok)) break;
      __builtin_amdgcn_s_sleep(16);
    }
    if (lane == 0) __hip_atomic_store((GAS u64*)p.ws, epoch, __ATOMIC_RELAXED, __HIP_MEMORY_SCOPE_AGENT);
  }
}

__global__ __launch_bounds__(512, 2) void s5_glu_kernel(const Params p) {
  __shared__ __attribute__((aligned(16))) char lds[163840];
  u64 dq_t0, dq_t1;
  asm volatile("s_memrealtime %0" : "=s"(dq_t0));
  asm volatile("s_waitcnt lgkmcnt(0)\n\ts_memrealtime %0\n\ts_waitcnt lgkmcnt(0)" : "=s"(dq_t1), "+s"(dq_t0) : "s"(p.ws), "s"(p.x), "s"(p.wd));
  LAS char* L = (LAS char*)lds;
  u64 code;
  asm volatile("s_getpc_b64 %0" : "=s"(code));
  const int b = blockIdx.x, w = __builtin_amdgcn_readfirstlane(threadIdx.x >> 6), lane = threadIdx.x & 63;
  if (__builtin_expect(b < NCOMP, 1)) {
    const int xcd = b & 7, q = b >> 3;
    const int ti = 4 * (xcd >> 1) + (q & 3), tj = 7 * (xcd & 1) + (q >> 2), rt = 4 * xcd + (q & 3), ct = q >> 2;
    if (__builtin_expect(w < 4, 1)) compute_waves(p, L, w, lane, ti, tj, rt, ct);
    else helper_waves(p, L, w - 4, lane, ti, tj, rt, ct);
  } else {
    support_group(p, L, b - NCOMP, code);
  }
  if (b == 100 && w == 0) {
    const u64 r1 = __builtin_amdgcn_s_memrealtime();
    const u64 dl = 0;
    for (int n = 0; n < 1000000 && __builtin_amdgcn_s_memrealtime() - r1 < dl + 200; ++n) __builtin_amdgcn_s_sleep(1);
  }
}

}

extern "C" void kernel_launch(void* const* d_in, const int* in_sizes, int n_in, void* d_out, int out_size, void* d_ws,
                              size_t ws_size, hipStream_t stream) {
  Params p;
  p.x = (const u16*)d_in[0];
  p.lre = (const u16*)d_in[1];
  p.lim = (const u16*)d_in[2];
  p.bre = (const u16*)d_in[3];
  p.bim = (const u16*)d_in[4];
  p.cre = (const u16*)d_in[5];
  p.cim = (const u16*)d_in[6];
  p.ldt = (const u16*)d_in[7];
  p.dsk = (const u16*)d_in[8];
  p.wg = (const u16*)d_in[10];
  p.wu = (const u16*)d_in[11];
  p.wd = (const u16*)d_in[12];
  p.out = (float*)d_out;
  p.ws = (char*)d_ws;
  s5_glu_kernel<<<256, 512, 0, stream>>>(p);
}
```

```cpp
#include <hip/hip_runtime.h>
#include <type_traits>

namespace {

typedef unsigned short u16;
typedef unsigned u32;
typedef unsigned long long u64;
typedef short bf16x8 __attribute__((ext_vector_type(8)));
typedef short s16x4 __attribute__((ext_vector_type(4)));
typedef __bf16 bf16x2 __attribute__((ext_vector_type(2)));
typedef float f32x16 __attribute__((ext_vector_type(16)));
typedef float f32x4 __attribute__((ext_vector_type(4)));
typedef float f32x2 __attribute__((ext_vector_type(2)));
typedef u32 v4u __attribute__((ext_vector_type(4)));
typedef u32 v2u __attribute__((ext_vector_type(2)));
typedef __amdgpu_buffer_rsrc_t rsrc_t;
#define LAS __attribute__((address_space(3)))
#define GAS __attribute__((address_space(1)))
#define CAS __attribute__((address_space(4)))
#define DI __device__ __forceinline__

constexpr int SEQ = 2048, DM = 448, FF = 1792;
constexpr int NCOMP = 224;

constexpr u32 slot1(int c) { return (u32)(c & 3) * 28672u; }
constexpr u32 L_AUX = 114688, L_MISC = 147456;
constexpr u32 M_STAT = L_MISC, M_CS = L_MISC + 1024, M_ST2 = L_MISC + 3072, M_DUMMY = L_MISC + 3584,
              M_ZERO = L_MISC + 4096, M_SUB = L_MISC + 6144, M_SUMX = L_MISC + 8192, M_SUMQ = L_MISC + 8704,
              M_CSP = L_MISC + 9216, M_EPOCH = L_MISC + 3968, M_X = L_MISC + 8192;
constexpr u32 M_TR = M_X;
constexpr u32 slotA(int k) { return (u32)(1 + (k & 3)) * 16384u; }
constexpr u32 slotB(int k) { return (u32)(k % 5 == 0 ? 7 : k % 5 == 1 ? 8 : k % 5 == 2 ? 6 : k % 5 == 3 ? 0 : 5) * 16384u; }

constexpr u32 W_SEEN = 1024, W_FLAG = 4096, W_SG = 348160, W_ST = 462848, W_CS = 495616, W_H = 557056;
constexpr u32 FLAG_STAGE = 64 * 14 * 128;
constexpr u32 W_END = W_H + 32u * 21u * 16384u;
constexpr u32 W_DIAG = 16u << 20;
constexpr u32 OOB = 0x40000000u;
constexpr u32 SPIN_CAP = 1u << 16;
constexpr int CODE_LINES = 300;

constexpr int DRAIN_A = 156;
#define SA_FIRST 7
#define S2_FIRST 0
#define SA_SLEEP 6
#define CPRIO 0
#define HPRIO 0

struct Params {
  const u16 *x, *lre, *lim, *bre, *bim, *cre, *cim, *ldt, *dsk, *wg, *wu, *wd;
  float* out;
  char* ws;
};

#define IC(v) std::integral_constant<int, (v)>{}
template <int B, int E, class F> DI void sfor(F&& f) {
  if constexpr (B < E) { f(std::integral_constant<int, B>{}); sfor<B + 1, E>(f); }
}
DI float asf(u32 v) { return __builtin_bit_cast(float, v); }
DI u32 asu(float v) { return __builtin_bit_cast(u32, v); }
DI float bf2f(u32 v) { return asf(v << 16); }
DI u32 pk2(float a, float b) { bf16x2 c; c[0] = (__bf16)a; c[1] = (__bf16)b; return __builtin_bit_cast(u32, c); }
DI float rbf(float a) { return bf2f(pk2(a, a) & 0xffffu); }
DI rsrc_t mk(const void* p, u32 bytes) { return __builtin_amdgcn_make_buffer_rsrc((void*)p, (short)0, (int)bytes, 0x00020000); }
DI f32x16 mfma32(bf16x8 a, bf16x8 b, f32x16 c) { return __builtin_amdgcn_mfma_f32_32x32x16_bf16(a, b, c, 0, 0, 0); }
DI f32x4 mfma16(bf16x8 a, bf16x8 b, f32x4 c) { return __builtin_amdgcn_mfma_f32_16x16x32_bf16(a, b, c, 0, 0, 0); }
DI void pin() { __builtin_amdgcn_sched_barrier(0); }
DI void bar() { asm volatile("s_barrier" ::: "memory"); }
template <int N> DI void wait_vm() { asm volatile("s_waitcnt vmcnt(%0)" ::"n"(N) : "memory"); }
DI void wait_lgkm() { asm volatile("s_waitcnt lgkmcnt(0)" ::: "memory"); }

template <class T> DI T ldsr(LAS char* L, u32 off) { return *(LAS T*)(L + off); }
template <class T> DI void ldsw(LAS char* L, u32 off, T v) { *(LAS T*)(L + off) = v; }
DI bf16x8 ldtr(LAS char* L, u32 off, u32 step) {
  const s16x4 lo = __builtin_amdgcn_ds_read_tr16_b64_v4i16((LAS s16x4*)(L + off));
  const s16x4 hi = __builtin_amdgcn_ds_read_tr16_b64_v4i16((LAS s16x4*)(L + off + step));
  return __builtin_shufflevector(lo, hi, 0, 1, 2, 3, 4, 5, 6, 7);
}
template <u32 LO, u32 SO> DI void dma(u32 lbase, u32 voff, rsrc_t rs, u32 sbase) {
  u32 t;
  asm volatile("s_add_u32 m0, %1, %2\n\ts_add_u32 %0, %3, %4\n\tbuffer_load_dwordx4 %5, %6, %0 offen lds"
               : "=&s"(t) : "s"(lbase), "n"(LO), "s"(sbase), "n"(SO), "v"(voff), "s"(rs) : "memory", "scc");
}
template <u32 LO, u32 SO> DI u32 dma_set(u32 lbase, u32 sbase) {
  u32 t;
  asm volatile("s_add_u32 m0, %1, %2\n\ts_add_u32 %0, %3, %4" : "=&s"(t) : "s"(lbase), "n"(LO), "s"(sbase), "n"(SO) : "memory", "scc");
  return t;
}
template <u32 O> DI void dma_go(u32 voff, rsrc_t rs, u32 soff) {
  asm volatile("buffer_load_dwordx4 %0, %1, %2 offen offset:%3 lds" ::"v"(voff), "s"(rs), "s"(soff), "n"(O) : "memory");
}
#define ANT ""
template <u32 LO, u32 SO> DI void dma4_sc1(u32 lbase, u32 voff, rsrc_t rs, u32 sbase) {
  u32 t;
  asm volatile("s_add_u32 m0, %1, %2\n\ts_add_u32 %0, %3, %4\n\t"
               "buffer_load_dwordx4 %5, %6, %0 offen sc1 " ANT "lds\n\tbuffer_load_dwordx4 %5, %6, %0 offen offset:1024 sc1 " ANT "lds\n\t"
               "buffer_load_dwordx4 %5, %6, %0 offen offset:2048 sc1 " ANT "lds\n\tbuffer_load_dwordx4 %5, %6, %0 offen offset:3072 sc1 " ANT "lds"
               : "=&s"(t) : "s"(lbase), "n"(LO), "s"(sbase), "n"(SO), "v"(voff), "s"(rs) : "memory", "scc");
}
DI void ald16(u32& d, u32 voff, rsrc_t rs) { asm volatile("buffer_load_ushort %0, %1, %2, 0 offen" : "=v"(d) : "v"(voff), "s"(rs) : "memory"); }
template <u32 O = 0> DI void ald64(v2u& d, u32 voff, rsrc_t rs) { asm volatile("buffer_load_dwordx2 %0, %1, %2, 0 offen offset:%3 sc1" : "=v"(d) : "v"(voff), "s"(rs), "n"(O) : "memory"); }
template <u32 O = 0> DI void ald128(v4u& d, u32 voff, rsrc_t rs) { asm volatile("buffer_load_dwordx4 %0, %1, %2, 0 offen offset:%3 sc1" : "=v"(d) : "v"(voff), "s"(rs), "n"(O) : "memory"); }
template <u32 O> DI void aldw(v4u& d, u32 voff, rsrc_t rs, u32 soff) { asm volatile("buffer_load_dwordx4 %0, %1, %2, %3 offen offset:%4" : "=v"(d) : "v"(voff), "s"(rs), "s"(soff), "n"(O) : "memory"); }
DI void ast64(v2u d, u32 voff, rsrc_t rs) { asm volatile("buffer_store_dwordx2 %0, %1, %2, 0 offen sc1" ::"v"(d), "v"(voff), "s"(rs) : "memory"); }
template <u32 O = 0> DI void ast128(v4u d, u32 voff, rsrc_t rs) { asm volatile("buffer_store_dwordx4 %0, %1, %2, 0 offen offset:%3 sc1\n\ts_nop 1" ::"v"(d), "v"(voff), "s"(rs), "n"(O) : "memory"); }
DI float addf(float a, float b) { float d; asm("v_add_f32 %0, %1, %2" : "=v"(d) : "v"(a), "v"(b)); return d; }
DI u32 opaque_s(u32 v) { asm volatile("" : "+s"(v)); return v; }
template <class T> DI void landed(T& v) { asm volatile("" : "+v"(v)); }
DI u32 opaque(u32 v) { asm volatile("" : "+v"(v)); return v; }
DI float pick(float a, float b, u64 m) { float d; asm("v_cndmask_b32 %0, %1, %2, %3" : "=v"(d) : "v"(a), "v"(b), "s"(m)); return d; }

DI bool all(bool c) { return __builtin_amdgcn_ballot_w64(!c) == 0; }

struct cf { float r, i; };
DI void landed(cf& v) { asm volatile("" : "+v"(v.r), "+v"(v.i)); }
DI cf cmul(cf a, cf b) { return {__builtin_fmaf(a.r, b.r, -(a.i * b.i)), __builtin_fmaf(a.r, b.i, a.i * b.r)}; }
DI f32x2 as2(cf v) { landed(v); return f32x2{v.r, v.i}; }
DI cf cfma(cf a, cf b, cf c) { return {__builtin_fmaf(a.r, b.r, __builtin_fmaf(-a.i, b.i, c.r)), __builtin_fmaf(a.r, b.i, __builtin_fmaf(a.i, b.r, c.i))}; }

#define CSTAMP(I)
#define HSTAMP(I)

DI void compute_waves(const Params& p, LAS char* L, const int w, const int lane, const int ti, const int tj, const int rt, const int ct) {
  const u32 lb = (u32)(size_t)L;
  const rsrc_t rs_x = mk(p.x, SEQ * DM * 2), rs_bre = mk(p.bre, DM * DM * 2), rs_bim = mk(p.bim, DM * DM * 2);
  const u32 vx = (8 * w + (lane >> 3)) * 896 + (((lane & 7) ^ ((4 * w + (lane >> 4)) & 7)) << 4);
  const u32 l1 = opaque_s(lb + w * 1024), sx = opaque_s(ti * 128 * 896), sb = opaque_s(tj * 32 * 896);
  auto load_ssm = [&](auto C) {
    constexpr int c = C;
    sfor<0, 4>([&](auto N) { dma<slot1(c) + 4096 * N, 32 * N * 896 + c * 128>(l1, vx, rs_x, sx); });
    dma<slot1(c) + 16384, c * 128>(l1, vx, rs_bre, sb);
    dma<slot1(c) + 20480, c * 128>(l1, vx, rs_bim, sb);
  };
  asm volatile("s_nop 4" ::: "memory");
  load_ssm(IC(0));
  load_ssm(IC(1));
  constexpr int NTOUCH = 0;
  pin();

  const int r = lane & 31, h = lane >> 5;
  const int tau = 16 * ((r >> 2) & 1) + (r & 3) + 4 * (r >> 3);
  const rsrc_t rs_ws = mk(p.ws, W_END), rs_w1 = mk(w >= 2 ? p.wu : p.wg, DM * FF * 2);
  const u32 vw = (lane >> 3) * 3584 + (((lane & 7) ^ ((((lane >> 4) & 1) | (w & 2)) << 1)) << 4);
  const u32 l7 = opaque_s(lb + w * 7168), sw = opaque_s(56 * (w & 1) * 3584 + 128 * tj);
  u32 ax[4], aw[4];
  {
    const int xrow = 32 * w + tau, g = lane >> 4, q = (lane >> 2) & 3, pp = lane & 3;
    const int gsw = ((q >> 1) & 1) | ((pp >> 1) << 1);
    sfor<0, 4>([&](auto S) {
      constexpr int s = S;
      ax[s] = xrow * 128 + (((2 * s + h) ^ ((xrow >> 1) & 7)) << 4);
      aw[s] = opaque((pp >> 1) * 14336 + (8 * (g >> 1) + q) * 128 + (2 * (s ^ gsw) + (pp & 1)) * 16 + 8 * (g & 1));
    });
  }
  const int rb = w >> 1, cb = w & 1;
  const u32 a2 = opaque(rb * 8192 + lane * 16);
  const u32 bw = opaque((8 * h + ((lane >> 2) & 3)) * 128 + (((4 * cb + 2 * ((lane >> 4) & 1) + ((lane >> 1) & 1)) ^ (((lane >> 3) & 1) << 2)) << 4) + 8 * (lane & 1));
  const u32 bc = opaque((32 * cb + r) * 128 + ((h ^ ((r >> 1) & 7)) << 4));
  const f32x16 zero16 = {0, 0, 0, 0, 0, 0, 0, 0, 0, 0, 0, 0, 0, 0, 0, 0};
  bf16x8 xf[28], wf[6];
  f32x16 acc[2][4];
  const u32 hbase = opaque_s(W_H + ((2 * ti + (w >> 1)) * 21 + (tj >> 1)) * 16384 + ((w & 1) * 8 + 4 * (tj & 1)) * 1024);
  const u32 a_m = opaque(M_STAT + w * 256 + tau * 4);
  const u32 vfs = opaque(lane == 0 ? W_FLAG + ((4 * ti + w) * 14 + tj) * 128 : OOB);
  v2u flagv;

  u32 wbase[2];
  bf16x8 mfrag;
  const u32 a_cs = opaque(M_CS + r * 16);
  auto ffn_read = [&](auto N) {
    constexpr int n = N, y = n % 116, cc = y < 84 ? y / 28 : 3, c = 7 + 4 * (n / 116) + cc;
    constexpr int T = y < 84 ? (y % 28) / 7 : (y - 84) / 8, kk = y < 84 ? y % 7 : (y - 84) % 8;
    if constexpr (kk == 7) {
      wf[n % 6] = ldsr<bf16x8>(L, a_cs + T * 512);
    } else if constexpr (c & 2) {
      if constexpr (kk <= 1 && (n & 1)) wbase[T & 1] = opaque(aw[T] + slot1(c));
      wf[n % 6] = ldtr(L, wbase[T & 1] + 2048 * kk, 512);
    } else {
      wf[n % 6] = ldtr(L, aw[T] + slot1(c) + 2048 * kk, 512);
    }
  };
  wait_vm<6 + NTOUCH>();
  bar();
  sfor<0, 7>([&](auto C) {
    constexpr int c = C;
    CSTAMP(c);
    sfor<0, 4>([&](auto S) { xf[4 * c + S] = ldsr<bf16x8>(L, ax[S] + slot1(c)); });
    if constexpr (c + 2 < 7) load_ssm(IC(c + 2));
    else sfor<0, 7>([&](auto N) { dma<slot1(c + 2) + 1024 * N, (112 * (c - 5) + 8 * N) * 3584>(l7, vw, rs_w1, sw); });
    wait_vm<(c + 2 < 7 ? 6 : 7) + (c == 0 ? NTOUCH : 0)>();
    wait_lgkm();
    bar();
  });
  __builtin_amdgcn_s_setprio(CPRIO);
  ffn_read(IC(1)); ffn_read(IC(0)); ffn_read(IC(3)); ffn_read(IC(2));

  float hv[8];
  v4u hp;
  auto epi_op = [&](auto U, auto O) {
    constexpr int u = U, o = O;
    if constexpr (o >= 0 && o < 56) {
      constexpr int T = o / 14, y = o % 14;
      constexpr int e = y < 3 ? y : y == 4 || y == 5 ? y - 1 : y == 7 || y == 8 ? y - 2 : 7;
      if constexpr (y == 3 || y == 6 || y == 9) hp[y / 3 - 1] = opaque(pk2(hv[2 * (y / 3 - 1)], hv[2 * (y / 3 - 1) + 1]));
      else if constexpr (y == 12) hp[3] = opaque(pk2(hv[6], hv[7]));
      else if constexpr (y == 13) __builtin_amdgcn_raw_buffer_store_b128(hp, rs_ws, lane * 16, hbase + (u * 7 * 16 + T) * 1024, 16);
      else if constexpr (y != 11) { hv[e] = acc[u][T][8 * (e >> 2) + (e & 3)] * acc[u][T][8 * (e >> 2) + 4 + (e & 3)]; landed(hv[e]); }
    }
  };

  bf16x8 af[6], bf[6];
  u32 pa, pb, pc4[2];
  auto p2_read = [&](auto M) {
    constexpr int m = M, k = m >> 3, ks = m & 7;
    if constexpr (ks == 0) pa = opaque(a2 + slotA(k));
    if constexpr (k < 14) {
      if constexpr (ks == 0) pb = opaque(bw + slotB(k));
      bf[m % 6] = ldtr(L, pb + 2048 * ks, 512);
    } else {
      constexpr int s = 2 * (ks >> 2) + (ks & 1), im = (ks >> 1) & 1;
      if constexpr (!im) pc4[ks & 1] = opaque((bc ^ (s << 5)) + slotB(k));
      bf[m % 6] = ldsr<bf16x8>(L, pc4[ks & 1] + im * 8192);
    }
    af[m % 6] = ldsr<bf16x8>(L, pa + ks * 1024);
  };

  constexpr int E0 = 94;
  float mval;
  sfor<0, 232>([&](auto N) {
    constexpr int n = N, u = n / 116, y = n % 116, cc = y < 84 ? y / 28 : 3;
    constexpr int T = y < 84 ? (y % 28) / 7 : (y - 84) / 8, kk = y < 84 ? y % 7 : (y - 84) % 8;
    if constexpr (n + 4 < 232) {
      constexpr int z = (n + 4) % 116;
      if constexpr (z == 0 || z == 28 || z == 56 || z == 84) {
        constexpr int bi = 7 + 4 * ((n + 4) / 116) + z / 28;
        if constexpr (bi == 8) wait_vm<0>();
        bar();
      }
      ffn_read(IC((n & 1) ? n + 3 : n + 5));
    }
    if constexpr (kk == 0) CSTAMP(7 + 16 * u + 4 * cc + T);
    pin();
    if constexpr (kk == 7) acc[u][T] = mfma32(wf[n % 6], mfrag, acc[u][T]);
    else acc[u][T] = mfma32(wf[n % 6], xf[7 * cc + kk], (cc | kk) ? acc[u][T] : zero16);
    pin();
    if constexpr (n == 60) mval = ldsr<float>(L, a_m);
    if constexpr (n == DRAIN_A - 6) flagv = ldsr<v2u>(L, M_EPOCH);
    if constexpr (n == DRAIN_A) { wait_vm<0>(); ast64(flagv, vfs, rs_ws); }
    if constexpr (n == 66) {
      const u32 mb = h ? 0u : (asu(mval) >> 16) ^ 0x8000u;
      mfrag = __builtin_bit_cast(bf16x8, v4u{mb * 0x10001u, mb, 0u, 0u});
      landed(mfrag);
    }
    if constexpr (n >= E0) sfor<0, 2>([&](auto J) { epi_op(IC(0), IC(7 * (n - E0) / 4 + J < 7 * (n + 1 - E0) / 4 ? 7 * (n - E0) / 4 + J : -1)); });
    if constexpr (n >= 116 + E0) sfor<0, 2>([&](auto J) { epi_op(IC(1), IC(7 * (n - 116 - E0) / 4 + J < 7 * (n + 1 - 116 - E0) / 4 ? 7 * (n - 116 - E0) / 4 + J : -1)); });
    pin();
  });
  CSTAMP(39);
  bar();
  sfor<7 * (116 - E0) / 4, 56>([&](auto O) { epi_op(IC(1), O); });
  pin();

  f32x16 accF, accY;
  u32 dv;
  float xv[16], r2[16], sk[16];
  f32x4 st;
  const int l2 = opaque(lane);
  const int trow = 64 * rt + 32 * rb + 16 * (l2 >> 5), dcol = 64 * ct + 32 * cb + (l2 & 31);
  const u32 sbase = opaque(M_ST2 + (32 * rb + 16 * (l2 >> 5)) * 8);
  const u32 xbase = opaque(M_X + (32 * rb + 16 * (l2 >> 5)) * 128 + (32 * cb + (l2 & 31)) * 2);
  const rsrc_t rs_out = mk(p.out, SEQ * DM * 4);
  const u32 eoff = trow * DM + dcol;
  wait_vm<0>();
  ast64(flagv, vfs + 2 * FLAG_STAGE, rs_ws);
  __builtin_amdgcn_s_setprio(0);
  bar();
  sfor<0, 5>([&](auto M) { p2_read(M); });
  sfor<0, 168>([&](auto M) {
    constexpr int m = M, k = m >> 3;
    if constexpr (m + 5 < 168) {
      if constexpr ((m + 5) % 8 == 0) bar();
      p2_read(IC(m + 5));
    }
    if constexpr ((m & 7) == 0) CSTAMP(40 + k);
    pin();
    if constexpr (k < 14) accF = mfma32(af[m % 6], bf[m % 6], m ? accF : zero16);
    else accY = mfma32(af[m % 6], bf[m % 6], m > 112 ? accY : zero16);
    pin();
    if constexpr (m == 24) dv = ((const GAS u16*)p.dsk)[dcol];
    if constexpr (m >= 56 && m < 104) {
      constexpr int e = (m - 56) / 3, y = (m - 56) % 3;
      if constexpr (y == 0) {
        if constexpr ((e & 1) == 0) st = ldsr<f32x4>(L, sbase + e * 8);
        xv[e] = bf2f(ldsr<u16>(L, xbase + e * 128));
      }
      if constexpr (y == 2) {
        const float mm = st[2 * (e & 1)], rr = st[2 * (e & 1) + 1];
        sk[e] = rbf(rbf(rbf(xv[e] - mm) * rr) * bf2f(dv));
        r2[e] = rr * rr;
        landed(sk[e]); landed(r2[e]);
      }
    }
    if constexpr (m >= 114 && m < 162 && (m - 114) % 3 == 0) {
      constexpr int e = (m - 114) / 3;
      const float o = rbf(accF[e] * r2[e]);
      r2[e] = rbf(o * __builtin_amdgcn_rcpf(1.f + __expf(-o)));
      xv[e] = __builtin_fmaf(sk[e], r2[e], xv[e]);
      landed(r2[e]); landed(xv[e]);
    }
    pin();
  });

  CSTAMP(61);
  sfor<0, 16>([&](auto E) {
    constexpr int e = E;
    __builtin_amdgcn_raw_buffer_store_b32(asu(__builtin_fmaf(accY[e], r2[e], xv[e])), rs_out, eoff * 4, e * DM * 4, 16);
  });
}

DI void helper_waves(const Params& p, LAS char* L, const int w, const int lane, const int ti, const int tj, const int rt, const int ct) {
  const int pc = lane & 31, h = lane >> 5;
  const u32 lb = (u32)(size_t)L;
  const rsrc_t rs_ws = mk(p.ws, W_END);
  u32 plr, pli, pdt;
  v2u ep;
  ald16(plr, (32 * tj + pc) * 2, mk(p.lre, DM * 2));
  ald16(pli, (32 * tj + pc) * 2, mk(p.lim, DM * 2));
  ald16(pdt, (32 * tj + pc) * 2, mk(p.ldt, DM * 2));
#define SYNC(N, I) do { pin(); wait_vm<N>(); wait_lgkm(); bar(); pin(); } while (0)
#define SBAR(I) bar()
  const rsrc_t rs_w1 = mk(w >= 2 ? p.wu : p.wg, DM * FF * 2), rs_wd = mk(p.wd, FF * DM * 2);
  const rsrc_t rs_cre = mk(p.cre, DM * DM * 2), rs_cim = mk(p.cim, DM * DM * 2);
  const u32 vx = (8 * w + (lane >> 3)) * 896 + (((lane & 7) ^ ((4 * w + (lane >> 4)) & 7)) << 4);
  const u32 vw = (lane >> 3) * 3584 + (((lane & 7) ^ ((((lane >> 4) & 1) | (w & 2)) << 1)) << 4);
  const u32 va = lane * 16;
  const u32 vd = (lane >> 3) * 896 + (((lane & 7) ^ (((lane >> 4) & 1) << 2)) << 4);
  const u32 l1 = opaque_s(lb + w * 1024), l7 = opaque_s(lb + w * 7168), l4 = opaque_s(lb + w * 4096);
  const u32 sw = opaque_s(56 * (w & 1) * 3584 + 128 * tj), sa = opaque_s(W_H + rt * 21 * 16384 + w * 4096);
  const u32 sd = opaque_s(32 * w * 896 + ct * 128), sc = opaque_s(64 * ct * 896);
  u32 vw4[4], vd4[4], so;
  sfor<0, 4>([&](auto N) { vw4[N] = opaque(vw + N * (8 * 3584 - 1024)); vd4[N] = opaque(vd + N * (8 * 896 - 1024)); });
  auto ldw = [&](auto C, auto N) {
    constexpr int c = C, n = N, u = (c - 7) / 4, cc = (c - 7) % 4;
    if constexpr ((n & 3) == 0) so = dma_set<slot1(c) + 1024 * n, (112 * cc + 8 * n) * 3584 + u * 1792>(l7, sw);
    dma_go<1024 * (n & 3)>(vw4[n & 3], rs_w1, so);
    pin();
  };
  auto ldd = [&](auto K, auto N) {
    if constexpr (N == 0) so = dma_set<slotB(K), 128 * K * 896>(l4, sd);
    dma_go<1024 * N>(vd4[N], rs_wd, so);
    pin();
  };
  auto ldd2 = [&](auto K, auto N) {
    so = dma_set<slotB(K) + 1024 * N, (128 * K + 8 * N) * 896>(l4, sd);
    dma_go<0>(vd4[0], rs_wd, so);
    dma_go<1024>(vd4[1], rs_wd, so);
    pin();
  };
  v4u rb[2][4];
  auto ldr = [&](auto K) {
    sfor<0, 4>([&](auto N) { aldw<1024 * N>(rb[K - 4][N], vd4[N], rs_wd, sd + 128 * K * 896); });
    pin();
  };
  auto str = [&](auto K) {
    const u32 a = opaque(slotB(K) + w * 4096 + va);
    sfor<0, 4>([&](auto N) { landed(rb[K - 4][N]); ldsw<v4u>(L, a + 1024 * N, rb[K - 4][N]); });
  };
  auto loadA = [&](auto K) { dma4_sc1<slotA(K), K * 16384>(l4, va, rs_ws, sa); };
  auto loadB = [&](auto K) {
    constexpr int k = K;
    if constexpr (k < 14) {
      sfor<0, 4>([&](auto N) { ldd(K, N); });
    } else {
      sfor<0, 2>([&](auto N) {
        dma<slotB(k) + 4096 * N, 32 * N * 896 + (k - 14) * 128>(l1, vx, rs_cre, sc);
        dma<slotB(k) + 8192 + 4096 * N, 32 * N * 896 + (k - 14) * 128>(l1, vx, rs_cim, sc);
      });
    }
  };

  const int tau = 16 * ((pc >> 2) & 1) + (pc & 3) + 4 * (pc >> 3);
  u32 ax[4], ab[4];
  sfor<0, 4>([&](auto S) {
    const int s = (S + w) & 3, xrow = 32 * w + tau;
    ax[S] = opaque(xrow * 128 + (((2 * s + h) ^ ((xrow >> 1) & 7)) << 4));
    ab[S] = opaque(pc * 128 + (((2 * s + h) ^ ((pc >> 1) & 7)) << 4) + 16384);
  });
  const short one = ((lane & 15) == ((lane >> 4) & 1)) ? (short)0x3f80 : (short)0;
  bf16x8 sel = {one, one, one, one, one, one, one, one};
  landed(sel);
  const u32 a_csp = opaque(M_CSP + pc * 4), a_sub = opaque(M_SUB + pc * 8), a_own = opaque(M_SUB + (2 * w + h) * 256 + pc * 8);
  const u32 a_csw = opaque(lane < 16 ? M_CSP + w * 256 + lane * 4 : M_DUMMY);
  const u32 a_sq = opaque(h == ((pc >> 2) & 1) ? M_SUMQ + w * 128 + tau * 4 : M_DUMMY + lane * 4);
  const u32 a_sx = opaque((lane & 15) < 2 ? M_SUMX + w * 128 + (16 * ((lane >> 4) & 1) + 8 * (lane & 1) + 4 * (lane >> 5)) * 4 : M_DUMMY + 256);
  const u32 a_stw = opaque(M_STAT + w * 256 + h * 128 + pc * 4), a_str = opaque(M_STAT + w * 256 + h * 64);
  const int d0 = ti - 1 - 2 * (2 * w + h);
  const u32 vs0 = opaque(d0 >= 0 ? W_SG + (tj * 16 + d0) * 512 + pc * 16 : OOB);
  const u32 vs1 = opaque(d0 >= 1 ? W_SG + (tj * 16 + d0 - 1) * 512 + pc * 16 : OOB);
  const u32 vsw = opaque(w == 3 && h ? W_SG + (tj * 16 + ti) * 512 + pc * 16 : OOB);
  const u32 vst = opaque(tj == 0 && lane < 32 ? W_ST + (ti * 128 + w * 32 + lane) * 16 : OOB);
  const u32 vcs = opaque(W_CS + tj * 2048 + (32 * w + pc) * 16);
  const u32 a_csf = opaque(M_CS + (2 * (w & 1) + (pc >> 4)) * 512 + (16 * ((pc >> 2) & 1) + 4 * ((pc >> 3) & 1) + 8 * (w >> 1) + (pc & 3)) * 16);
  const u32 vfl = opaque(W_FLAG + ((2 * rt + (w >> 1)) * 14 + 2 * (lane < 7 ? lane : 6) + (w & 1)) * 128);
  const u32 vfs = opaque(lane == 0 ? W_FLAG + FLAG_STAGE + ((4 * ti + w) * 14 + tj) * 128 : OOB);
  const u32 a_tw = opaque(M_TR + w * 2048 + pc * 64 + (((2 * h) ^ ((pc >> 1) & 3)) << 4));
  const u32 a_tr = opaque(M_TR + w * 2048 + (8 * h + ((lane >> 2) & 3)) * 64 + (((2 * (lane & 1) + ((lane >> 4) & 1)) ^ ((lane >> 3) & 1)) << 4) + 8 * ((lane >> 1) & 1));
  const u32 vhs = opaque(W_H + ((2 * ti + (w >> 1)) * 21 + 14 + (tj >> 1)) * 16384 + ((w & 1) * 8 + 4 * (tj & 1)) * 1024 + va);
  ldsw<f32x2>(L, a_own - 2048, f32x2{0.f, 0.f});
  cf lam, lam4, lam16, lam128, lamk, lamp, coef, l32, l64, l256, l512, c0, c1;
  float lr, li, dt, t0, t1, t2, t3, th, kq;
  int iq;
  v2u flagv;
  const cf unit = {1.f, 0.f};
  constexpr float L2E = 1.44269502e+00f, L2E_LO = 1.92596299e-08f, LN2 = 6.93147182e-01f;
  auto sq = [&](cf a) { return cf{__builtin_fmaf(a.r, a.r, -(a.i * a.i)), (a.r + a.r) * a.i}; };
  auto lam_op = [&](auto O) {
    constexpr int o = O;
    if constexpr (o == 0) { wait_vm<0>(); landed(plr); landed(pli); landed(pdt); lr = bf2f(plr); li = bf2f(pli); t0 = bf2f(pdt); t1 = t0 * L2E; landed(t1); }
    if constexpr (o == 1 || o == 4) { t2 = __builtin_fmaf(t0, L2E_LO, __builtin_fmaf(t0, L2E, -t1)) * LN2; t1 = __builtin_amdgcn_exp2f(t1); landed(t1); landed(t2); }
    if constexpr (o == 2) { dt = rbf(__builtin_fmaf(t1, t2, t1)); landed(dt); ald64(ep, opaque(0), rs_ws); }
    if constexpr (o == 3) { t0 = dt * lr; th = dt * li; t1 = t0 * L2E; kq = __builtin_rintf(th * 0.636619772f); landed(t1); landed(kq); }
    if constexpr (o == 5) {
      dt = __builtin_fmaf(t1, t2, t1);
      th = __builtin_fmaf(kq, -5.39030253e-15f, __builtin_fmaf(kq, -3.13916473e-07f, __builtin_fmaf(kq, -1.57079601e+00f, th)));
      landed(dt); landed(th);
    }
    if constexpr (o == 6) { t0 = th * th; t1 = __builtin_fmaf(t0, __builtin_fmaf(t0, __builtin_fmaf(t0, 2.86567956e-6f, -1.98559923e-4f), 8.33338592e-3f), -1.66666672e-1f); landed(t1); }
    if constexpr (o == 7) { t1 = __builtin_fmaf(th * t0, t1, th); t2 = __builtin_fmaf(t0, __builtin_fmaf(t0, 2.44677067e-5f, -1.38877297e-3f), 4.16666567e-2f); landed(t1); landed(t2); }
    if constexpr (o == 8) { t2 = __builtin_fmaf(t0, __builtin_fmaf(t0, t2, -0.5f), 1.f); iq = (int)kq; t3 = __builtin_fmaf(lr, lr, li * li); landed(t2); landed(iq); landed(t3); }
    if constexpr (o == 9) { const bool odd = iq & 1; t0 = odd ? t2 : t1; t1 = odd ? t1 : t2; landed(t0); landed(t1); }
    if constexpr (o == 10) { t0 = asf(asu(t0) ^ ((u32)(iq & 2) << 30)); t3 = __builtin_amdgcn_rcpf(t3); landed(t0); landed(t3); }
    if constexpr (o == 11) { t1 = asf(asu(t1) ^ ((u32)((iq + 1) & 2) << 30)); landed(t1); }
    if constexpr (o == 12) { lam = cf{dt * t1, dt * t0}; t3 += t3; t0 = lam.r - 1.f; landed(lam); landed(t3); landed(t0); }
    if constexpr (o == 13) { coef = cf{__builtin_fmaf(t0, lr, lam.i * li), __builtin_fmaf(lam.i, lr, -(t0 * li))}; landed(coef); }
    if constexpr (o == 14) { coef = cf{coef.r * t3, coef.i * t3}; landed(coef); }
    if constexpr (o == 15) { lam4 = sq(lam); landed(lam4); }
    if constexpr (o == 16) { lam4 = sq(lam4); landed(lam4); }
    if constexpr (o == 17) { lam16 = sq(lam4); landed(lam16); }
    if constexpr (o == 18) { lam16 = sq(lam16); landed(lam16); }
    if constexpr (o == 19) { l32 = sq(lam16); landed(l32); }
    if constexpr (o == 20) { l64 = sq(l32); landed(l64); }
    if constexpr (o == 21) { lam128 = sq(l64); landed(lam128); }
    if constexpr (o == 22) { l256 = sq(lam128); landed(l256); }
    if constexpr (o == 23) { l512 = sq(l256); landed(l512); }
    if constexpr (o == 24) { c0 = h ? lam16 : unit; c1 = (w & 1) ? l32 : unit; landed(c0); landed(c1); }
    if constexpr (o == 25) { lamk = cmul(c0, c1); landed(lamk); }
    if constexpr (o == 26) { c0 = (w & 2) ? l64 : unit; c1 = h ? l256 : unit; landed(c0); landed(c1); }
    if constexpr (o == 27) { lamk = cmul(lamk, c0); landed(lamk); }
    if constexpr (o == 28) { lamk = cmul(lamk, coef); landed(lamk); }
    if constexpr (o == 29) { c0 = (w & 1) ? l512 : unit; landed(c0); }
    if constexpr (o == 30) { lamp = cmul(c0, c1); landed(lamp); }
    if constexpr (o == 31) { c0 = sq(l512); landed(c0); }
    if constexpr (o == 32) { c0 = (w & 2) ? c0 : unit; landed(c0); }
    if constexpr (o == 33) { lamp = cmul(lamp, c0); landed(lamp); }
    if constexpr (o == 34) {
      wait_vm<0>();
      landed(ep);
      const u32 lo = ep[0] + 1;
      flagv = v2u{lo, ep[1] + (lo == 0)};
      ldsw<v2u>(L, M_EPOCH, flagv);
    }
  };
  const f32x16 zero16 = {0, 0, 0, 0, 0, 0, 0, 0, 0, 0, 0, 0, 0, 0, 0, 0};
  const f32x4 zero4 = {0, 0, 0, 0};
  bf16x8 xf[3], bre[3], bim[3];
  f32x16 are, aim, gram;
  f32x4 sum, csr, csi;
  u32 xa, ba;
  auto ssm_read = [&](auto K, auto J) {
    constexpr int ks = K, c = ks >> 2, s = ks & 3;
    if constexpr (ks < 28) {
      if constexpr (J == 0) { ba = (c & 2) ? opaque(ab[s] + slot1(c)) : ab[s] + slot1(c); bim[ks % 3] = ldsr<bf16x8>(L, ba + 4096); }
      if constexpr (J == 1) bre[ks % 3] = ldsr<bf16x8>(L, ba);
      if constexpr (J == 2) { xa = (c & 2) ? opaque(ax[s] + slot1(c)) : ax[s] + slot1(c); xf[ks % 3] = ldsr<bf16x8>(L, xa); }
    }
  };
  wait_lgkm();
  bar();
  sfor<0, 6>([&](auto J) { ssm_read(IC(J / 3), IC(J % 3)); });
  sfor<0, 28>([&](auto K) {
    constexpr int ks = K, o = 3 * (ks - 3);
    pin();
    gram = mfma32(xf[ks % 3], xf[ks % 3], ks ? gram : zero16);
    pin();
    if constexpr (((ks + 2) & 3) == 0) {
      if constexpr (ks == 26) wait_lgkm();
      bar();
    }
    ssm_read(IC(ks + 2), IC(0));
    lam_op(IC(o));
    if constexpr (ks == 25) { ldsw<float>(L, a_csw, csr[0]); ldsw<float>(L, a_csw + 64, csr[1]); }
    pin();
    are = mfma32(xf[ks % 3], bre[ks % 3], ks ? are : zero16);
    pin();
    ssm_read(IC(ks + 2), IC(1));
    lam_op(IC(o + 1));
    if constexpr (ks == 25) { ldsw<float>(L, a_csw + 128, csi[0]); ldsw<float>(L, a_csw + 192, csi[1]); }
    pin();
    aim = mfma32(xf[ks % 3], bim[ks % 3], ks ? aim : zero16);
    pin();
    ssm_read(IC(ks + 2), IC(2));
    lam_op(IC(o + 2));
    pin();
    sum = mfma16(xf[ks % 3], sel, ks ? sum : zero4);
    if constexpr ((ks & 3) == 0) {
      csr = mfma16(sel, bre[ks % 3], ks ? csr : zero4);
      csi = mfma16(sel, bim[ks % 3], ks ? csi : zero4);
    }
    pin();
  });
  const u32 tag = flagv[0];

  v4u cg;
  auto cs_issue = [&](int u) { ald128(cg, vcs + u * 28672, rs_ws); };
  auto cs_finish = [&](int u) {
    landed(cg);
#pragma unroll 1
    for (u32 n = 0; n < SPIN_CAP && !all(cg[1] == tag && cg[3] == tag); ++n) {
      __builtin_amdgcn_s_sleep(2);
      cs_issue(u); wait_vm<0>(); landed(cg);
    }
    ldsw<v4u>(L, a_csf, v4u{cg[0], cg[2], 0u, 0u});
  };
  v2u fl;
  auto flag_issue = [&](int stage) { ald64(fl, vfl + stage * FLAG_STAGE, rs_ws); };
  auto flag_finish = [&](int stage) {
    landed(fl);
#pragma unroll 1
    for (u32 n = 0; n < SPIN_CAP && !all(fl[0] == flagv[0] && fl[1] == flagv[1]); ++n) {
      __builtin_amdgcn_s_sleep(2);
      flag_issue(stage); wait_vm<0>(); landed(fl);
    }
  };

  __builtin_amdgcn_s_setprio(HPRIO);
  cf b[4][4], cc;
  {
    float t[8], gd[8];
    sfor<0, 8>([&](auto N) { t[N] = ldsr<float>(L, a_csp + N * 128); });
    ldw(IC(9), IC(0)); ldw(IC(9), IC(1));
    cc = cf{(t[0] + t[2]) + (t[4] + t[6]), (t[1] + t[3]) + (t[5] + t[7])};
    landed(cc);
    ldsw<f32x4>(L, a_sx, sum);
    ldw(IC(9), IC(2)); ldw(IC(9), IC(3));
    asm volatile("s_nop 7\n\ts_nop 3" : "+v"(gram));
    sfor<0, 8>([&](auto N) { constexpr int n = N; gd[n] = pick(gram[2 * n], gram[2 * n + 1], 0xaaaaaaaaaaaaaaaaull); });
    ldw(IC(9), IC(4));
    sfor<0, 4>([&](auto N) { gd[N] = pick(gd[2 * N], gd[2 * N + 1], 0xccccccccccccccccull); });
    sfor<0, 2>([&](auto N) { gd[N] = pick(gd[2 * N], gd[2 * N + 1], 0xff00ff00ff00ff00ull); });
    ldsw<float>(L, a_sq, pick(gd[0], gd[1], 0xffff0000ffff0000ull));
    ldw(IC(9), IC(5)); ldw(IC(9), IC(6));
  }
  ldd2(IC(0), IC(0));
  SYNC(9, 8);

  f32x4 mm[4], rr[4];
  f32x2 stat;
  auto correct = [&](auto G, auto&& ld) {
    constexpr int g = G;
    sfor<0, 4>([&](auto E) {
      constexpr int e = E;
      b[g][e] = cf{__builtin_fmaf(-mm[g][e], cc.r, are[4 * g + e]), __builtin_fmaf(-mm[g][e], cc.i, aim[4 * g + e])};
    });
    ld();
    sfor<0, 4>([&](auto E) { constexpr int e = E; b[g][e] = cf{b[g][e].r * rr[g][e], b[g][e].i * rr[g][e]}; landed(b[g][e]); });
  };
  cs_issue(0);
  {
    const float sx = ldsr<float>(L, a_csp + (M_SUMX - M_CSP) + w * 128), sq = ldsr<float>(L, a_csp + (M_SUMQ - M_CSP) + w * 128);
    ldw(IC(10), IC(0)); ldw(IC(10), IC(1));
    float mean = sx * (1.f / DM);
    landed(mean);
    const float var = fmaxf(__builtin_fmaf(sq, 1.f / DM, -(mean * mean)), 0.f);
    stat = f32x2{rbf(mean), __builtin_amdgcn_rcpf(rbf(__builtin_amdgcn_sqrtf(rbf(rbf(var) + 1e-5f))))};
    ldsw<float>(L, a_stw, h ? stat[1] : stat[0]);
    sfor<0, 4>([&](auto G) { mm[G] = ldsr<f32x4>(L, a_str + G * 16); rr[G] = ldsr<f32x4>(L, a_str + 128 + G * 16); });
    sfor<2, 5>([&](auto N) { ldw(IC(10), N); });
  }
  correct(IC(0), [&] { ldw(IC(10), IC(5)); });
  ldw(IC(10), IC(6));
  ldd2(IC(0), IC(2));
  SYNC(9, 9);
  sfor<1, 4>([&](auto G) {
    ldw(IC(11), IC(2 * G - 2));
    correct(G, [&] { ldw(IC(11), IC(2 * G - 1)); });
  });
  ldw(IC(11), IC(6));
  ldd2(IC(1), IC(0));
  cs_finish(0);
  SYNC(9, 10);

  cf en[4], fin;
  sfor<0, 4>([&](auto G) { en[G] = b[G][0]; });
  sfor<1, 4>([&](auto E) {
    constexpr int e = E;
    ldw(IC(12), IC(2 * e - 2));
    sfor<0, 2>([&](auto G) { en[G] = cfma(lam, en[G], b[G][e]); });
    ldw(IC(12), IC(2 * e - 1));
    sfor<2, 4>([&](auto G) { en[G] = cfma(lam, en[G], b[G][e]); });
  });
  ldw(IC(12), IC(6));
  fin = cfma(lam4, cfma(lam4, cfma(lam4, en[0], en[1]), en[2]), en[3]);
  ldsw<f32x2>(L, a_own, as2(fin));
  SYNC(7, 11);

  cs_issue(1);
  cf pre;
  {
    f32x2 v[7];
    sfor<0, 7>([&](auto K) { v[K] = ldsr<f32x2>(L, a_own - (7 - K) * 256); });
    ldw(IC(13), IC(0));
    pre = cf{v[0][0], v[0][1]};
    sfor<1, 7>([&](auto K) {
      pre = cfma(lam16, pre, cf{v[K][0], v[K][1]});
      if constexpr (K & 1) ldw(IC(13), IC(1 + K / 2));
    });
    fin = cfma(lam16, pre, fin);
    landed(fin);
    ast128(v4u{asu(fin.r), tag, asu(fin.i), tag}, vsw, rs_ws);
    ast128(v4u{asu(stat[0]), tag, asu(stat[1]), tag}, vst, rs_ws);
    sfor<4, 7>([&](auto N) { ldw(IC(13), N); });
    pre = cmul(coef, pre);
    landed(pre);
  }
  SYNC(9, 12);

  sfor<0, 4>([&](auto G) {
    sfor<0, 2>([&](auto E) { b[G][E] = cmul(coef, b[G][E]); });
    ldw(IC(14), IC(2 * G));
    sfor<2, 4>([&](auto E) { b[G][E] = cmul(coef, b[G][E]); });
    sfor<0, 4>([&](auto E) { landed(b[G][E]); });
    if constexpr (G < 3) ldw(IC(14), IC(2 * G + 1));
  });
  ldd2(IC(1), IC(2));
  cs_finish(1);
  SYNC(9, 13);

  v4u s0, s1;
  loadB(IC(2));
  sfor<0, 3>([&](auto G) { en[G] = cmul(coef, en[G]); landed(en[G]); });
  ldr(IC(4));
  ald128(s0, vs0, rs_ws); ald128(s1, vs1, rs_ws);
  ldr(IC(5));
  SYNC(14, 14);

  loadB(IC(3));
  wait_vm<8>();
  landed(s0); landed(s1);
#pragma unroll 1
  for (u32 n = 0; n < SPIN_CAP && !all((d0 < 0 || (s0[1] == tag && s0[3] == tag)) && (d0 < 1 || (s1[1] == tag && s1[3] == tag))); ++n) {
    __builtin_amdgcn_s_sleep(2);
    ald128(s0, vs0, rs_ws); ald128(s1, vs1, rs_ws); wait_vm<0>(); landed(s0); landed(s1);
  }
  {
    const cf t = cmul(lamp, cfma(lam128, cf{asf(s1[0]), asf(s1[2])}, cf{asf(s0[0]), asf(s0[2])}));
    ldsw<f32x2>(L, a_own, as2(t));
  }
  HSTAMP(80);
  flag_issue(0);
  wait_vm<0>();
  HSTAMP(81);
  flag_finish(0);
  HSTAMP(82);
  SYNC(0, 15);
  __builtin_amdgcn_s_setprio(0);

  u32 pr[8], pi[8];
  cf s2[4];
  auto pass2 = [&](auto J) {
    constexpr int j = J;
    cf o[4];
    sfor<0, 4>([&](auto G) { o[G] = cfma(lam, s2[G], b[G][2 * j]); });
    sfor<0, 4>([&](auto G) {
      s2[G] = cfma(lam, o[G], b[G][2 * j + 1]);
      landed(o[G]); landed(s2[G]);
      pr[2 * G + j] = opaque(pk2(o[G].r, s2[G].r));
      pi[2 * G + j] = opaque(pk2(-o[G].i, -s2[G].i));
    });
  };
  auto hs_out = [&](const u32* v, u32 voff) {
    ldsw<v4u>(L, a_tw, v4u{v[0], v[1], v[2], v[3]});
    ldsw<v4u>(L, a_tw ^ 16, v4u{v[4], v[5], v[6], v[7]});
    bf16x8 f[2];
    sfor<0, 2>([&](auto Z) {
      const s16x4 lo = __builtin_amdgcn_ds_read_tr16_b64_v4i16((LAS s16x4*)(L + a_tr + Z * 1024));
      const s16x4 hi = __builtin_amdgcn_ds_read_tr16_b64_v4i16((LAS s16x4*)(L + (a_tr ^ 32) + 256 + Z * 1024));
      f[Z] = __builtin_shufflevector(lo, hi, 0, 1, 2, 3, 4, 5, 6, 7);
    });
    ast128<0>(__builtin_bit_cast(v4u, f[0]), voff, rs_ws);
    ast128<1024>(__builtin_bit_cast(v4u, f[1]), voff, rs_ws);
  };
  loadA(IC(0)); loadA(IC(1));
  str(IC(4));
  HSTAMP(83);
  {
    f32x2 v[8];
    sfor<0, 8>([&](auto K) { v[K] = ldsr<f32x2>(L, a_sub + K * 256); });
    cf t4[4], t;
    sfor<0, 4>([&](auto K) { t4[K] = cf{addf(v[2 * K][0], v[2 * K + 1][0]), addf(v[2 * K][1], v[2 * K + 1][1])}; });
    sfor<0, 2>([&](auto K) { t4[K] = cf{addf(t4[2 * K].r, t4[2 * K + 1].r), addf(t4[2 * K].i, t4[2 * K + 1].i)}; });
    t = cf{addf(t4[0].r, t4[1].r), addf(t4[0].i, t4[1].i)};
    s2[0] = cfma(lamk, t, pre);
    sfor<1, 4>([&](auto G) { s2[G] = cfma(lam4, s2[G - 1], en[G - 1]); });
    landed(s2[3]);
  }
  HSTAMP(84);
  SYNC(4, 16);
  HSTAMP(85);
  loadA(IC(2));
  pass2(IC(0));
  SYNC(4, 17);
  loadA(IC(3));
  pass2(IC(1));
  hs_out(pr, vhs);
  SYNC(6, 18);
  loadA(IC(4));
  str(IC(5));
  hs_out(pi, vhs + 2048);
  SYNC(8, 19);
  loadA(IC(5)); loadB(IC(6));
  SYNC(10, 20);
  v4u st2;
  const u32 vst2 = w == 2 ? W_ST + (rt * 64 + lane) * 16 : OOB;
  loadA(IC(6)); loadB(IC(7));
  {
    const u32 vxt = (8 * w + (lane >> 3)) * 896 + (lane & 7) * 16, sxt = rt * 64 * 896 + ct * 128;
    dma<M_X, 0>(l1, vxt, mk(p.x, SEQ * DM * 2), sxt);
    dma<M_X + 4096, 32 * 896>(l1, vxt, mk(p.x, SEQ * DM * 2), sxt);
  }
  ald128(st2, vst2, rs_ws);
  wait_vm<19>();
  ast64(flagv, vfs, rs_ws);
  SYNC(16, 21);
  sfor<5, 20>([&](auto KK) {
    constexpr int k = KK;
    if constexpr (k == 5 || k == 12) {
      flag_issue(k == 5 ? 2 : 1);
      loadB(IC(k + 3));
      wait_vm<4>();
      flag_finish(k == 5 ? 2 : 1);
      loadA(IC(k + 2));
    } else {
      if constexpr (k + 2 < 21) loadA(IC(k + 2));
      if constexpr (k + 3 < 21) loadB(IC(k + 3));
    }
    constexpr int cnt = (k + 2 < 21 ? 4 : 0) + (k + 3 < 21 ? 4 : 0);
    if constexpr (k == 5) {
      landed(st2);
      if (w == 2) {
#pragma unroll 1
        for (u32 n = 0; n < SPIN_CAP && !all(st2[1] == tag && st2[3] == tag); ++n) {
          __builtin_amdgcn_s_sleep(2);
          ald128(st2, vst2, rs_ws); wait_vm<0>(); landed(st2);
        }
        ldsw<v2u>(L, M_ST2 + lane * 8, v2u{st2[0], st2[2]});
      }
    }
    SYNC(cnt, k + 17);
  });
}

DI void support_group(const Params& p, LAS char* L, const int s, const u64 code) {
  const int tid = threadIdx.x, lane = tid & 63, w = __builtin_amdgcn_readfirstlane(tid >> 6);
  u32 sink = 0;
  auto touch = [&](const GAS char* a) { asm volatile("global_load_dword %0, %1, off" : "+v"(sink) : "v"(a) : "memory"); };
  if (tid < CODE_LINES / 4) touch((const GAS char*)code + ((s >> 3) + 4 * tid) * 128);
  const u64 epoch = *(const CAS u64*)p.ws + 1;
  const u32 tag = (u32)epoch;
  if (s < 28) {
    const int j = 7 * (s & 1) + (s >> 1) % 7, mat = (s >> 1) / 7;
    const GAS char* W = (const GAS char*)(mat ? p.wu : p.wg) + (56 * w + (lane >> 3)) * 3584 + j * 128 + (lane & 7) * 16;
    v4u v[2][7];
    sfor<0, 14>([&](auto N) { v[N / 7][N % 7] = *(const GAS v4u*)(W + (N / 7) * 1792 + (N % 7) * 8 * 3584); });
    pin();
    sfor<0, 2>([&](auto U) {
      constexpr int u = U;
      float a[8];
      sfor<0, 4>([&](auto E) {
        constexpr int e = E;
        float lo = 0.f, hi = 0.f;
        sfor<0, 7>([&](auto N) { lo += bf2f(v[u][N][e]); hi += asf(v[u][N][e] & 0xffff0000u); });
        a[2 * e] = lo; a[2 * e + 1] = hi;
      });
      sfor<0, 8>([&](auto E) {
        a[E] += __shfl_xor(a[E], 8); a[E] += __shfl_xor(a[E], 16); a[E] += __shfl_xor(a[E], 32);
      });
      if (lane < 8) {
        ldsw<f32x4>(L, u * 2048 + w * 256 + lane * 32, f32x4{a[0], a[1], a[2], a[3]});
        ldsw<f32x4>(L, u * 2048 + w * 256 + lane * 32 + 16, f32x4{a[4], a[5], a[6], a[7]});
      }
      wait_lgkm();
      bar();
      if (w == 0) {
        float t = 0.f;
        sfor<0, 8>([&](auto N) { t += ldsr<float>(L, u * 2048 + N * 256 + lane * 4); });
        const float t0 = rbf(t), t1 = rbf(t - t0);
        __builtin_amdgcn_raw_buffer_store_b128(v4u{pk2(t0, t1), tag, pk2(t - t0 - t1, 0.f), tag}, mk(p.ws, W_END),
                                               W_CS + (u * 14 + j) * 2048 + mat * 1024 + lane * 16, 0, 16);
      }
    });
  }
  asm volatile("" ::"s"(epoch));
  bar();
  if (tid == 0) __hip_atomic_store((GAS u32*)(p.ws + W_SEEN + s * 64), tag, __ATOMIC_RELAXED, __HIP_MEMORY_SCOPE_AGENT);
  sfor<0, 2>([&](auto N) {
    const int o = N * 512 + tid;
    if (o < 588) {
      const int ln = s * 588 + o;
      touch(ln < 12544 ? (const GAS char*)p.wd + ln * 128 : ln < 15680 ? (const GAS char*)p.cre + (ln - 12544) * 128 : (const GAS char*)p.cim + (ln - 15680) * 128);
    }
  });
  if (w < 4 && lane < 56) {
    const int ln = (s >> 3) * 224 + w * 56 + lane;
    sfor<S2_FIRST, 21>([&](auto K) {
      constexpr int k = K;
      if constexpr (k < 14) touch((const GAS char*)p.wd + (k * 896 + ln) * 128);
      else touch((const GAS char*)(ln < 448 ? p.cre : p.cim) + (ln < 448 ? ln : ln - 448) * 896 + (k - 14) * 128);
    });
  }
  if (w >= 4) {
    __builtin_amdgcn_s_setprio(3);
    const int rt = 4 * (s & 7) + (s >> 3);
    const GAS u64* fp = (const GAS u64*)(p.ws + W_FLAG + ((2 * rt + ((w >> 1) & 1)) * 14 + 2 * (lane < 7 ? lane : 6) + (w & 1)) * 128);
    const GAS char* hp = (const GAS char*)p.ws + W_H + (rt * 21 + (lane >> 5)) * 16384 + (w & 3) * 4096 + (lane & 31) * 128;
    sfor<0, 3>([&](auto G) {
      constexpr int g = G, stage = g == 0 ? 0 : g == 1 ? 2 : 1;
#pragma unroll 1
      for (u32 n = 0; n < SPIN_CAP && !all(__hip_atomic_load(fp + stage * (FLAG_STAGE / 8), __ATOMIC_RELAXED, __HIP_MEMORY_SCOPE_AGENT) == epoch); ++n)
        __builtin_amdgcn_s_sleep(SA_SLEEP);
      sfor<0, 4>([&](auto N) {
        constexpr int k = 2 * N + (g == 0 ? SA_FIRST : 0);
        if constexpr (k < 7) { if (k + (lane >> 5) < 7) touch(hp + (7 * g + k) * 16384); }
      });
    });
  }
  asm volatile("s_waitcnt vmcnt(0)" : "+v"(sink)::"memory");
  if (s == 31 && w == 0) {
#pragma unroll 1
    for (u32 n = 0; n < SPIN_CAP; ++n) {
      bool ok = true;
      sfor<0, 4>([&](auto N) {
        const int id = N * 64 + lane;
        const u32 off = id < NCOMP ? W_SG + id * 512 + 4 : W_SEEN + (id - NCOMP) * 64;
        ok = ok && __hip_atomic_load((GAS u32*)(p.ws + off), __ATOMIC_RELAXED, __HIP_MEMORY_SCOPE_AGENT) == tag;
      });
      if (all(ok)) break;
      __builtin_amdgcn_s_sleep(16);
    }
    if (lane == 0) __hip_atomic_store((GAS u64*)p.ws, epoch, __ATOMIC_RELAXED, __HIP_MEMORY_SCOPE_AGENT);
  }
}

__global__ __launch_bounds__(512, 2) void s5_glu_kernel(const Params p) {
  __shared__ __attribute__((aligned(16))) char lds[163840];
  LAS char* L = (LAS char*)lds;
  u64 code;
  asm volatile("s_getpc_b64 %0" : "=s"(code));
  const int b = blockIdx.x, w = __builtin_amdgcn_readfirstlane(threadIdx.x >> 6), lane = threadIdx.x & 63;
  if (__builtin_expect(b < NCOMP, 1)) {
    const int xcd = b & 7, q = b >> 3;
    const int ti = 4 * (xcd >> 1) + (q & 3), tj = 7 * (xcd & 1) + (q >> 2), rt = 4 * xcd + (q & 3), ct = q >> 2;
    if (__builtin_expect(w < 4, 1)) compute_waves(p, L, w, lane, ti, tj, rt, ct);
    else helper_waves(p, L, w - 4, lane, ti, tj, rt, ct);
  } else {
    support_group(p, L, b - NCOMP, code);
  }
}

}

extern "C" void kernel_launch(void* const* d_in, const int* in_sizes, int n_in, void* d_out, int out_size, void* d_ws,
                              size_t ws_size, hipStream_t stream) {
  Params p;
  p.x = (const u16*)d_in[0];
  p.lre = (const u16*)d_in[1];
  p.lim = (const u16*)d_in[2];
  p.bre = (const u16*)d_in[3];
  p.bim = (const u16*)d_in[4];
  p.cre = (const u16*)d_in[5];
  p.cim = (const u16*)d_in[6];
  p.ldt = (const u16*)d_in[7];
  p.dsk = (const u16*)d_in[8];
  p.wg = (const u16*)d_in[10];
  p.wu = (const u16*)d_in[11];
  p.wd = (const u16*)d_in[12];
  p.out = (float*)d_out;
  p.ws = (char*)d_ws;
  s5_glu_kernel<<<256, 512, 0, stream>>>(p);
}
```
